```python
import torch

def _softmax(x, axis=-1):
    return torch.softmax(x, dim=axis)
def _gelu(x):
    return torch.nn.functional.gelu(x, approximate="tanh")
def _silu(x):
    return torch.nn.functional.silu(x)
def _sigmoid(x):
    return torch.sigmoid(x)
def _layernorm(x, axis=-1, eps=1e-5):
    m = x.mean(dim=axis, keepdim=True)
    v = x.var(dim=axis, keepdim=True, unbiased=False)
    return (x - m) / torch.sqrt(v + eps)
def _scatter_add(tgt, idx, src, axis=0):
    return tgt.clone().index_add_(axis, idx, src)
def _segment_sum(src, seg, n):
    out = torch.zeros((n,) + src.shape[1:], dtype=src.dtype)
    return out.index_add_(0, seg, src)
def _pad(x, pw):
    flat = tuple(v for pair in reversed(pw) for v in pair)
    return torch.nn.functional.pad(x, flat)

kernel_name = 'sequential_preLN__sliding_block__swiglu_ffn__prompt'

def setup_inputs():

    _w = (lambda a: (lambda t: t.to(torch.bfloat16) if t.is_floating_point() else t)(torch.as_tensor(a)))
    def _A_setup():
        B, S, D = 1, 2048, 448
        return {
            "x": _w((lambda *s: torch.randn(*s, dtype=torch.bfloat16) * 0.05)(B, S, D)),
            "wqkv": _w((lambda *s: torch.randn(*s, dtype=torch.bfloat16) * 0.05)(D, 3 * D)),
            "wo": _w((lambda *s: torch.randn(*s, dtype=torch.bfloat16) * 0.05)(D, D)),
        }
    _A_in = _A_setup()

    def _B_setup():
        B, S, D, F = 1, 2048, 448, 1792
        return {
            "x": _w((lambda *s: torch.randn(*s, dtype=torch.bfloat16) * 0.05)(B, S, D)),
            "w_gate": _w((lambda *s: torch.randn(*s, dtype=torch.bfloat16) * 0.05)(D, F)),
            "w_up": _w((lambda *s: torch.randn(*s, dtype=torch.bfloat16) * 0.05)(D, F)),
            "w_down": _w((lambda *s: torch.randn(*s, dtype=torch.bfloat16) * 0.05)(F, D)),
        }
    _B_in = _B_setup()

    return {
        "A_x": _A_in["x"],
        "A_wqkv": _A_in["wqkv"],
        "A_wo": _A_in["wo"],
        "B_x": _B_in["x"],
        "B_w_gate": _B_in["w_gate"],
        "B_w_up": _B_in["w_up"],
        "B_w_down": _B_in["w_down"],
    }

def reference(A_x, A_wqkv, A_wo, B_x, B_w_gate, B_w_up, B_w_down):
    def _A_ref(x, wqkv, wo):
        B, S, D = x.shape
        H = 16; Dh = D // H; W = 256
        qkv = (lambda x, *d: x.permute(*d))((x @ wqkv).reshape(B, S, 3, H, Dh), 2, 0, 3, 1, 4)
        q, k, v = qkv[0], qkv[1], qkv[2]
        scores = torch.einsum("bhqd,bhkd->bhqk", q, k) * (Dh ** -0.5)
        idx = torch.arange(S)
        mask = (idx[:, None] - idx[None, :] >= 0) & (idx[:, None] - idx[None, :] < W)
        scores = torch.where(mask[None, None], scores, -1e9)
        p = _softmax(scores, axis=-1)
        o = torch.einsum("bhqk,bhkd->bhqd", p, v)
        return (lambda x, *d: x.permute(*d))(o, 0, 2, 1, 3).reshape(B, S, D) @ wo

    def _B_ref(x, w_gate, w_up, w_down):
        g = _silu(x @ w_gate)
        u = x @ w_up
        return (g * u) @ w_down

    x = A_x
    h = x + _A_ref(_layernorm(x, axis=-1), A_wqkv, A_wo)
    h2 = h + _B_ref(_layernorm(h, axis=-1), B_w_gate, B_w_up, B_w_down)
    return h2
```

```cpp
#include <hip/hip_runtime.h>
#include <hip/hip_bf16.h>
#include <stdint.h>

typedef unsigned short u16;
__device__ __forceinline__ float bf2f(u16 x) { return __uint_as_float(((unsigned)x) << 16); }
__device__ __forceinline__ u16 f2bf(float f) { __hip_bfloat16 b = __float2bfloat16(f); return *(u16*)&b; }

constexpr int S = 2048, D = 448, H = 16, DH = 28, W = 256, F = 1792;

__global__ void ln_kernel(const u16* __restrict__ x, u16* __restrict__ y) {
    int row = blockIdx.x; int t = threadIdx.x;
    float v[7]; float s = 0;
    for (int i = 0; i < 7; i++) { v[i] = bf2f(x[row * D + t + 64 * i]); s += v[i]; }
    for (int o = 32; o; o >>= 1) s += __shfl_xor(s, o);
    float m = s / D; float q = 0;
    for (int i = 0; i < 7; i++) { float d = v[i] - m; q += d * d; }
    for (int o = 32; o; o >>= 1) q += __shfl_xor(q, o);
    float r = rsqrtf(q / D + 1e-5f);
    for (int i = 0; i < 7; i++) y[row * D + t + 64 * i] = f2bf((v[i] - m) * r);
}
__global__ void gemm_kernel(const u16* __restrict__ A, const u16* __restrict__ B, const u16* __restrict__ res, u16* __restrict__ C, int M, int N, int K) {
    int n = blockIdx.x * 64 + threadIdx.x; int m = blockIdx.y;
    if (n >= N) return;
    float acc = 0;
    for (int k = 0; k < K; k++) acc += bf2f(A[m * K + k]) * bf2f(B[k * N + n]);
    if (res) acc = bf2f(f2bf(acc)) + bf2f(res[m * N + n]);
    C[m * N + n] = f2bf(acc);
}
__global__ void attn_kernel(const u16* __restrict__ qkv, u16* __restrict__ o) {
    int q = blockIdx.x, h = blockIdx.y, t = threadIdx.x;
    __shared__ float p[256]; __shared__ float red[4];
    int key = q - t; float s = -1e30f;
    if (key >= 0) {
        float a = 0;
        for (int d = 0; d < DH; d++) a += bf2f(qkv[q * 3 * D + h * DH + d]) * bf2f(qkv[key * 3 * D + D + h * DH + d]);
        s = a * rsqrtf((float)DH);
    }
    float m = s;
    for (int o2 = 32; o2; o2 >>= 1) m = fmaxf(m, __shfl_xor(m, o2));
    if ((t & 63) == 0) red[t >> 6] = m;
    __syncthreads();
    m = fmaxf(fmaxf(red[0], red[1]), fmaxf(red[2], red[3]));
    __syncthreads();
    float e = key >= 0 ? __expf(s - m) : 0.f;
    float l = e;
    for (int o2 = 32; o2; o2 >>= 1) l += __shfl_xor(l, o2);
    if ((t & 63) == 0) red[t >> 6] = l;
    p[t] = e;
    __syncthreads();
    l = red[0] + red[1] + red[2] + red[3];
    if (t < DH) {
        float a = 0;
        for (int j = 0; j < 256 && q - j >= 0; j++) a += p[j] * bf2f(qkv[(q - j) * 3 * D + 2 * D + h * DH + t]);
        o[q * D + h * DH + t] = f2bf(a / l);
    }
}
__global__ void act_kernel(const u16* __restrict__ g, const u16* __restrict__ u, u16* __restrict__ a, int n) {
    int i = blockIdx.x * 256 + threadIdx.x;
    if (i < n) { float gv = bf2f(g[i]); float s = gv / (1.f + __expf(-gv)); a[i] = f2bf(bf2f(f2bf(s)) * bf2f(u[i])); }
}

extern "C" void kernel_launch(void* const* d_in, const int* in_sizes, int n_in,
                              void* d_out, int out_size, void* d_ws, size_t ws_size,
                              hipStream_t stream) {
    const u16* x = (const u16*)d_in[0];
    const u16* wqkv = (const u16*)d_in[1];
    const u16* wo = (const u16*)d_in[2];
    const u16* wg = (const u16*)d_in[4];
    const u16* wu = (const u16*)d_in[5];
    const u16* wd = (const u16*)d_in[6];
    u16* out = (u16*)d_out;
    u16* ws = (u16*)d_ws;
    u16* xn = ws; u16* qkv = xn + S * D; u16* o = qkv + S * 3 * D; u16* h = o + S * D; u16* hn = h + S * D;
    u16* g = hn + S * D; u16* u = g + S * F; u16* a = u + S * F;
    ln_kernel<<<S, 64, 0, stream>>>(x, xn);
    gemm_kernel<<<dim3(3 * D / 64, S), 64, 0, stream>>>(xn, wqkv, nullptr, qkv, S, 3 * D, D);
    attn_kernel<<<dim3(S, H), 256, 0, stream>>>(qkv, o);
    gemm_kernel<<<dim3(D / 64, S), 64, 0, stream>>>(o, wo, x, h, S, D, D);
    ln_kernel<<<S, 64, 0, stream>>>(h, hn);
    gemm_kernel<<<dim3(F / 64, S), 64, 0, stream>>>(hn, wg, nullptr, g, S, F, D);
    gemm_kernel<<<dim3(F / 64, S), 64, 0, stream>>>(hn, wu, nullptr, u, S, F, D);
    act_kernel<<<S * F / 256, 256, 0, stream>>>(g, u, a, S * F);
    gemm_kernel<<<dim3(D / 64, S), 64, 0, stream>>>(a, wd, h, out, S, D, F);
}
```

```cpp
#include <hip/hip_runtime.h>
#include <hip/hip_bf16.h>
#include <stdint.h>
#include <stdio.h>

typedef unsigned short u16;
__device__ __forceinline__ float bf2f(u16 x) { return __uint_as_float(((unsigned)x) << 16); }
__device__ __forceinline__ u16 f2bf(float f) { __hip_bfloat16 b = __float2bfloat16(f); return *(u16*)&b; }

constexpr int S = 2048, D = 448, H = 16, DH = 28, W = 256, F = 1792;

__global__ void ln_kernel(const u16* __restrict__ x, u16* __restrict__ y) {
    int row = blockIdx.x; int t = threadIdx.x;
    float v[7]; float s = 0;
    for (int i = 0; i < 7; i++) { v[i] = bf2f(x[row * D + t + 64 * i]); s += v[i]; }
    for (int o = 32; o; o >>= 1) s += __shfl_xor(s, o);
    float m = s / D; float q = 0;
    for (int i = 0; i < 7; i++) { float d = v[i] - m; q += d * d; }
    for (int o = 32; o; o >>= 1) q += __shfl_xor(q, o);
    float r = rsqrtf(q / D + 1e-5f);
    for (int i = 0; i < 7; i++) y[row * D + t + 64 * i] = f2bf((v[i] - m) * r);
}
__global__ void gemm_kernel(const u16* __restrict__ A, const u16* __restrict__ B, const u16* __restrict__ res, u16* __restrict__ C, int M, int N, int K) {
    int n = blockIdx.x * 64 + threadIdx.x; int m = blockIdx.y;
    if (n >= N) return;
    float acc = 0;
    for (int k = 0; k < K; k++) acc += bf2f(A[m * K + k]) * bf2f(B[k * N + n]);
    if (res) acc = bf2f(f2bf(acc)) + bf2f(res[m * N + n]);
    C[m * N + n] = f2bf(acc);
}
__global__ void attn_kernel(const u16* __restrict__ qkv, u16* __restrict__ o) {
    int q = blockIdx.x, h = blockIdx.y, t = threadIdx.x;
    __shared__ float p[256]; __shared__ float red[4];
    int key = q - t; float s = -1e30f;
    if (key >= 0) {
        float a = 0;
        for (int d = 0; d < DH; d++) a += bf2f(qkv[q * 3 * D + h * DH + d]) * bf2f(qkv[key * 3 * D + D + h * DH + d]);
        s = a * rsqrtf((float)DH);
    }
    float m = s;
    for (int o2 = 32; o2; o2 >>= 1) m = fmaxf(m, __shfl_xor(m, o2));
    if ((t & 63) == 0) red[t >> 6] = m;
    __syncthreads();
    m = fmaxf(fmaxf(red[0], red[1]), fmaxf(red[2], red[3]));
    __syncthreads();
    float e = key >= 0 ? __expf(s - m) : 0.f;
    float l = e;
    for (int o2 = 32; o2; o2 >>= 1) l += __shfl_xor(l, o2);
    if ((t & 63) == 0) red[t >> 6] = l;
    p[t] = e;
    __syncthreads();
    l = red[0] + red[1] + red[2] + red[3];
    if (t < DH) {
        float a = 0;
        for (int j = 0; j < 256 && q - j >= 0; j++) a += p[j] * bf2f(qkv[(q - j) * 3 * D + 2 * D + h * DH + t]);
        o[q * D + h * DH + t] = f2bf(a / l);
    }
}
__global__ void act_kernel(const u16* __restrict__ g, const u16* __restrict__ u, u16* __restrict__ a, int n) {
    int i = blockIdx.x * 256 + threadIdx.x;
    if (i < n) { float gv = bf2f(g[i]); float s = gv / (1.f + __expf(-gv)); a[i] = f2bf(bf2f(f2bf(s)) * bf2f(u[i])); }
}


typedef unsigned u32;
typedef unsigned long long u64;
typedef u32 u32x4 __attribute__((ext_vector_type(4)));
__global__ void __launch_bounds__(512, 2) probe(const char* __restrict__ x, const char* __restrict__ wd, u32* out, u64* stamps) {
    int lane = threadIdx.x & 63; int w = __builtin_amdgcn_readfirstlane(threadIdx.x >> 6);
    int b = blockIdx.x; int xcd = b & 7, s = b >> 3;
    auto r = __builtin_amdgcn_make_buffer_rsrc((void*)x, 0, 2048 * 896, 0x00020000);
    auto r2 = __builtin_amdgcn_make_buffer_rsrc((void*)wd, 0, 1792 * 896, 0x00020000);
    u64 t0 = __builtin_amdgcn_s_memtime();
    u32x4 acc = {0, 0, 0, 0};
    {
        int T = 4 * (xcd >> 1) + (s >> 3);
        u32x4 v[14];
        int off = (T * 128 + w * 16 + (lane >> 2)) * 896 + (lane & 3) * 16;
        #pragma unroll
        for (int i = 0; i < 14; i++) v[i] = __builtin_amdgcn_raw_buffer_load_b128(r, off, i * 64, 0);
        #pragma unroll
        for (int i = 0; i < 14; i++) acc ^= v[i];
    }
    u32 xx = acc.x ^ acc.y ^ acc.z ^ acc.w;
    asm volatile("s_waitcnt vmcnt(0)" :: "v"(xx) : "memory");
    __syncthreads();
    u64 t1 = __builtin_amdgcn_s_memtime();
    {
        int T = (4 * (xcd >> 1) + (s >> 3)) % 14;
        u32x4 v[14];
        int off = (T * 128 + w * 16 + (lane >> 2)) * 896 + (lane & 3) * 16;
        #pragma unroll
        for (int i = 0; i < 14; i++) v[i] = __builtin_amdgcn_raw_buffer_load_b128(r2, off, i * 64, 0);
        #pragma unroll
        for (int i = 0; i < 14; i++) acc ^= v[i];
    }
    xx ^= acc.x ^ acc.y ^ acc.z ^ acc.w;
    asm volatile("s_waitcnt vmcnt(0)" :: "v"(xx) : "memory");
    __syncthreads();
    u64 t2 = __builtin_amdgcn_s_memtime();
    out[b * 512 + threadIdx.x] = xx;
    asm volatile("s_waitcnt vmcnt(0)" ::: "memory");
    __syncthreads();
    u64 t3 = __builtin_amdgcn_s_memtime();
    if (threadIdx.x == 0) { stamps[b] = t1 - t0; stamps[256 + b] = t2 - t1; stamps[512 + b] = t3 - t2; }
}
__global__ void report(u64* st) {
    u64 mn[3], mx[3], sm[3];
    for (int j = 0; j < 3; j++) { mn[j] = ~0ull; mx[j] = 0; sm[j] = 0; for (int i = 0; i < 256; i++) { u64 v = st[j * 256 + i]; mn[j] = v < mn[j] ? v : mn[j]; mx[j] = v > mx[j] ? v : mx[j]; sm[j] += v; } }
    printf("PROBE x: %llu %llu %llu | wd: %llu %llu %llu | st: %llu %llu %llu\n", mn[0], sm[0] / 256, mx[0], mn[1], sm[1] / 256, mx[1], mn[2], sm[2] / 256, mx[2]);
}

extern "C" void kernel_launch(void* const* d_in, const int* in_sizes, int n_in,
                              void* d_out, int out_size, void* d_ws, size_t ws_size,
                              hipStream_t stream) {
    const u16* x = (const u16*)d_in[0];
    const u16* wqkv = (const u16*)d_in[1];
    const u16* wo = (const u16*)d_in[2];
    const u16* wg = (const u16*)d_in[4];
    const u16* wu = (const u16*)d_in[5];
    const u16* wd = (const u16*)d_in[6];
    u16* out = (u16*)d_out;
    u16* ws = (u16*)d_ws;
    u16* xn = ws; u16* qkv = xn + S * D; u16* o = qkv + S * 3 * D; u16* h = o + S * D; u16* hn = h + S * D;
    u16* g = hn + S * D; u16* u = g + S * F; u16* a = u + S * F;
    u32* po = (u32*)((char*)d_ws + (128 << 20)); u64* pst = (u64*)((char*)d_ws + (160 << 20));
    probe<<<256, 512, 0, stream>>>((const char*)x, (const char*)wd, po, pst);
    report<<<1, 1, 0, stream>>>(pst);
    ln_kernel<<<S, 64, 0, stream>>>(x, xn);
    gemm_kernel<<<dim3(3 * D / 64, S), 64, 0, stream>>>(xn, wqkv, nullptr, qkv, S, 3 * D, D);
    attn_kernel<<<dim3(S, H), 256, 0, stream>>>(qkv, o);
    gemm_kernel<<<dim3(D / 64, S), 64, 0, stream>>>(o, wo, x, h, S, D, D);
    ln_kernel<<<S, 64, 0, stream>>>(h, hn);
    gemm_kernel<<<dim3(F / 64, S), 64, 0, stream>>>(hn, wg, nullptr, g, S, F, D);
    gemm_kernel<<<dim3(F / 64, S), 64, 0, stream>>>(hn, wu, nullptr, u, S, F, D);
    act_kernel<<<S * F / 256, 256, 0, stream>>>(g, u, a, S * F);
    gemm_kernel<<<dim3(D / 64, S), 64, 0, stream>>>(a, wd, h, out, S, D, F);
}
```

```cpp
#define SEL 0
#define AMP 0
#include <hip/hip_runtime.h>
#include <hip/hip_bf16.h>
#include <stdint.h>
#include <stdio.h>

typedef unsigned short u16;
__device__ __forceinline__ float bf2f(u16 x) { return __uint_as_float(((unsigned)x) << 16); }
__device__ __forceinline__ u16 f2bf(float f) { __hip_bfloat16 b = __float2bfloat16(f); return *(u16*)&b; }

constexpr int S = 2048, D = 448, H = 16, DH = 28, W = 256, F = 1792;

__global__ void ln_kernel(const u16* __restrict__ x, u16* __restrict__ y) {
    int row = blockIdx.x; int t = threadIdx.x;
    float v[7]; float s = 0;
    for (int i = 0; i < 7; i++) { v[i] = bf2f(x[row * D + t + 64 * i]); s += v[i]; }
    for (int o = 32; o; o >>= 1) s += __shfl_xor(s, o);
    float m = s / D; float q = 0;
    for (int i = 0; i < 7; i++) { float d = v[i] - m; q += d * d; }
    for (int o = 32; o; o >>= 1) q += __shfl_xor(q, o);
    float r = rsqrtf(q / D + 1e-5f);
    for (int i = 0; i < 7; i++) y[row * D + t + 64 * i] = f2bf((v[i] - m) * r);
}
__global__ void gemm_kernel(const u16* __restrict__ A, const u16* __restrict__ B, const u16* __restrict__ res, u16* __restrict__ C, int M, int N, int K) {
    int n = blockIdx.x * 64 + threadIdx.x; int m = blockIdx.y;
    if (n >= N) return;
    float acc = 0;
    for (int k = 0; k < K; k++) acc += bf2f(A[m * K + k]) * bf2f(B[k * N + n]);
    if (res) acc = bf2f(f2bf(acc)) + bf2f(res[m * N + n]);
    C[m * N + n] = f2bf(acc);
}
__global__ void attn_kernel(const u16* __restrict__ qkv, u16* __restrict__ o) {
    int q = blockIdx.x, h = blockIdx.y, t = threadIdx.x;
    __shared__ float p[256]; __shared__ float red[4];
    int key = q - t; float s = -1e30f;
    if (key >= 0) {
        float a = 0;
        for (int d = 0; d < DH; d++) a += bf2f(qkv[q * 3 * D + h * DH + d]) * bf2f(qkv[key * 3 * D + D + h * DH + d]);
        s = a * rsqrtf((float)DH);
    }
    float m = s;
    for (int o2 = 32; o2; o2 >>= 1) m = fmaxf(m, __shfl_xor(m, o2));
    if ((t & 63) == 0) red[t >> 6] = m;
    __syncthreads();
    m = fmaxf(fmaxf(red[0], red[1]), fmaxf(red[2], red[3]));
    __syncthreads();
    float e = key >= 0 ? __expf(s - m) : 0.f;
    float l = e;
    for (int o2 = 32; o2; o2 >>= 1) l += __shfl_xor(l, o2);
    if ((t & 63) == 0) red[t >> 6] = l;
    p[t] = e;
    __syncthreads();
    l = red[0] + red[1] + red[2] + red[3];
    if (t < DH) {
        float a = 0;
        for (int j = 0; j < 256 && q - j >= 0; j++) a += p[j] * bf2f(qkv[(q - j) * 3 * D + 2 * D + h * DH + t]);
        o[q * D + h * DH + t] = f2bf(a / l);
    }
}
__global__ void act_kernel(const u16* __restrict__ g, const u16* __restrict__ u, u16* __restrict__ a, int n) {
    int i = blockIdx.x * 256 + threadIdx.x;
    if (i < n) { float gv = bf2f(g[i]); float s = gv / (1.f + __expf(-gv)); a[i] = f2bf(bf2f(f2bf(s)) * bf2f(u[i])); }
}


typedef unsigned u32;
typedef unsigned long long u64;
typedef u32 u32x4 __attribute__((ext_vector_type(4)));
__global__ void __launch_bounds__(512, 2) probe(const char* __restrict__ x, const char* __restrict__ wd, u32* out, u64* stamps) {
    int lane = threadIdx.x & 63; int w = __builtin_amdgcn_readfirstlane(threadIdx.x >> 6);
    int b = blockIdx.x; int xcd = b & 7, s = b >> 3;
    auto r = __builtin_amdgcn_make_buffer_rsrc((void*)x, 0, 2048 * 896, 0x00020000);
    auto r2 = __builtin_amdgcn_make_buffer_rsrc((void*)wd, 0, 1792 * 896, 0x00020000);
    u64 t0 = __builtin_amdgcn_s_memtime();
    u32x4 acc = {0, 0, 0, 0};
    {
        int T = 4 * (xcd >> 1) + (s >> 3);
        u32x4 v[14];
        int off = (T * 128 + w * 16 + (lane >> 2)) * 896 + (lane & 3) * 16;
        #pragma unroll
        for (int i = 0; i < 14; i++) v[i] = __builtin_amdgcn_raw_buffer_load_b128(r, off, i * 64, 0);
        #pragma unroll
        for (int i = 0; i < 14; i++) acc ^= v[i];
    }
    u32 xx = acc.x ^ acc.y ^ acc.z ^ acc.w;
    asm volatile("s_waitcnt vmcnt(0)" :: "v"(xx) : "memory");
    __syncthreads();
    u64 t1 = __builtin_amdgcn_s_memtime();
    {
        int T = (4 * (xcd >> 1) + (s >> 3)) % 14;
        u32x4 v[14];
        int off = (T * 128 + w * 16 + (lane >> 2)) * 896 + (lane & 3) * 16;
        #pragma unroll
        for (int i = 0; i < 14; i++) v[i] = __builtin_amdgcn_raw_buffer_load_b128(r2, off, i * 64, 0);
        #pragma unroll
        for (int i = 0; i < 14; i++) acc ^= v[i];
    }
    xx ^= acc.x ^ acc.y ^ acc.z ^ acc.w;
    asm volatile("s_waitcnt vmcnt(0)" :: "v"(xx) : "memory");
    __syncthreads();
    u64 t2 = __builtin_amdgcn_s_memtime();
    out[b * 512 + threadIdx.x] = xx;
    asm volatile("s_waitcnt vmcnt(0)" ::: "memory");
    __syncthreads();
    u64 t3 = __builtin_amdgcn_s_memtime();
    if (threadIdx.x == 0) { stamps[b] = t1 - t0; stamps[256 + b] = t2 - t1; stamps[512 + b] = t3 - t2; }
}
__global__ void report(u64* st) {
    u64 sm = 0; for (int i = 0; i < 256; i++) sm += st[SEL * 256 + i];
    sm = sm / 256; if (sm > 30000) sm = 30000;
    u64 n = sm * AMP;
    u64 t0 = __builtin_amdgcn_s_memrealtime();
    while (__builtin_amdgcn_s_memrealtime() - t0 < n) __builtin_amdgcn_s_sleep(8);
}
extern "C" void kernel_launch(void* const* d_in, const int* in_sizes, int n_in,
                              void* d_out, int out_size, void* d_ws, size_t ws_size,
                              hipStream_t stream) {
    const u16* x = (const u16*)d_in[0];
    const u16* wqkv = (const u16*)d_in[1];
    const u16* wo = (const u16*)d_in[2];
    const u16* wg = (const u16*)d_in[4];
    const u16* wu = (const u16*)d_in[5];
    const u16* wd = (const u16*)d_in[6];
    u16* out = (u16*)d_out;
    u16* ws = (u16*)d_ws;
    u16* xn = ws; u16* qkv = xn + S * D; u16* o = qkv + S * 3 * D; u16* h = o + S * D; u16* hn = h + S * D;
    u16* g = hn + S * D; u16* u = g + S * F; u16* a = u + S * F;
    u32* po = (u32*)((char*)d_ws + (128 << 20)); u64* pst = (u64*)((char*)d_ws + (160 << 20));
    probe<<<256, 512, 0, stream>>>((const char*)x, (const char*)wd, po, pst);
    report<<<1, 1, 0, stream>>>(pst);
    ln_kernel<<<S, 64, 0, stream>>>(x, xn);
    gemm_kernel<<<dim3(3 * D / 64, S), 64, 0, stream>>>(xn, wqkv, nullptr, qkv, S, 3 * D, D);
    attn_kernel<<<dim3(S, H), 256, 0, stream>>>(qkv, o);
    gemm_kernel<<<dim3(D / 64, S), 64, 0, stream>>>(o, wo, x, h, S, D, D);
    ln_kernel<<<S, 64, 0, stream>>>(h, hn);
    gemm_kernel<<<dim3(F / 64, S), 64, 0, stream>>>(hn, wg, nullptr, g, S, F, D);
    gemm_kernel<<<dim3(F / 64, S), 64, 0, stream>>>(hn, wu, nullptr, u, S, F, D);
    act_kernel<<<S * F / 256, 256, 0, stream>>>(g, u, a, S * F);
    gemm_kernel<<<dim3(D / 64, S), 64, 0, stream>>>(a, wd, h, out, S, D, F);
}
```

```cpp
#include <hip/hip_runtime.h>
#include <stdint.h>

typedef unsigned u32;
typedef unsigned long long u64;
typedef u32 u32x2 __attribute__((ext_vector_type(2)));
typedef u32 u32x4 __attribute__((ext_vector_type(4)));
typedef float f32x2 __attribute__((ext_vector_type(2)));
typedef float f32x4 __attribute__((ext_vector_type(4)));
typedef __bf16 bf16x2 __attribute__((ext_vector_type(2)));
typedef __bf16 bf16x8 __attribute__((ext_vector_type(8)));
typedef short s16x4 __attribute__((ext_vector_type(4)));
typedef short s16x8 __attribute__((ext_vector_type(8)));
#define LAS __attribute__((address_space(3)))
#define CAS __attribute__((address_space(4)))
#define DEVI __device__ __forceinline__

constexpr u32 WS_EPOCH = 0;
constexpr u32 WS_FA = 4096;
constexpr u32 WS_FB = WS_FA + 2048;
constexpr u32 WS_FC = WS_FB + 2048;
constexpr u32 WS_FD = WS_FC + 2048;
constexpr u32 WS_QK = 65536;
constexpr u32 WS_V = WS_QK + 128 * 112 * 256;
constexpr u32 WS_O = WS_V + 256 * 448 * 16;
constexpr u32 WS_H = WS_O + 128 * 16 * 1024;
constexpr u32 WS_ACT = WS_H + 128 * 14 * 1024;
constexpr u32 WS_END = WS_ACT + 128 * 56 * 1024;
constexpr u32 WS_STAMP = WS_END;

constexpr u32 L_SLOT = 28672;
constexpr u32 L_WO = 57344;
constexpr u32 L_GUHI = 114688;
constexpr u32 L_SCR = 157696;
constexpr u32 L_K = 0, L_V = 24576;

__shared__ __attribute__((aligned(1024))) char smem[163840];

DEVI f32x4 mfma(u32x4 a, u32x4 b, f32x4 c) {
    return __builtin_amdgcn_mfma_f32_16x16x32_bf16(__builtin_bit_cast(bf16x8, a), __builtin_bit_cast(bf16x8, b), c, 0, 0, 0);
}
DEVI u32x4 trfrag(u32 a0, u32 a1) {
    s16x4 lo = __builtin_amdgcn_ds_read_tr16_b64_v4i16((LAS s16x4*)(smem + a0));
    s16x4 hi = __builtin_amdgcn_ds_read_tr16_b64_v4i16((LAS s16x4*)(smem + a1));
    return __builtin_bit_cast(u32x4, __builtin_shufflevector(lo, hi, 0, 1, 2, 3, 4, 5, 6, 7));
}
DEVI u32x4 lds128(u32 a) { return *(LAS u32x4*)(smem + a); }
DEVI void lds128w(u32 a, u32x4 v) { *(LAS u32x4*)(smem + a) = v; }
DEVI f32x4 ldsf4(u32 a) { return *(LAS f32x4*)(smem + a); }
DEVI void ldsf4w(u32 a, f32x4 v) { *(LAS f32x4*)(smem + a) = v; }
DEVI u32 pk2(float a, float b) { f32x2 f = {a, b}; return __builtin_bit_cast(u32, __builtin_convertvector(f, bf16x2)); }
DEVI float dot2(u32 a, u32 b, float c) { return __builtin_amdgcn_fdot2_f32_bf16(__builtin_bit_cast(bf16x2, a), __builtin_bit_cast(bf16x2, b), c, false); }
DEVI float bflo(u32 v) { return __builtin_bit_cast(float, v << 16); }
DEVI float bfhi(u32 v) { return __builtin_bit_cast(float, v & 0xffff0000u); }
DEVI float asf(u32 v) { return __builtin_bit_cast(float, v); }
DEVI u32 asu(float v) { return __builtin_bit_cast(u32, v); }
DEVI float xsum(float v) {
    u32 u = __builtin_bit_cast(u32, v);
    auto s = __builtin_amdgcn_permlane16_swap(u, u, false, false);
    v = asf(s[0]) + asf(s[1]);
    u = __builtin_bit_cast(u32, v);
    auto t = __builtin_amdgcn_permlane32_swap(u, u, false, false);
    return asf(t[0]) + asf(t[1]);
}
DEVI float xmax(float v) {
    u32 u = __builtin_bit_cast(u32, v);
    auto s = __builtin_amdgcn_permlane16_swap(u, u, false, false);
    v = fmaxf(asf(s[0]), asf(s[1]));
    u = __builtin_bit_cast(u32, v);
    auto t = __builtin_amdgcn_permlane32_swap(u, u, false, false);
    return fmaxf(asf(t[0]), asf(t[1]));
}
DEVI u32x4 mksrd(const void* p, u32 bytes) {
    u64 a = (u64)p;
    u32x4 r = {(u32)a, (u32)(a >> 32) & 0xffffu, bytes, 0x00020000u};
    return r;
}
DEVI void dma(u32 lds, u32 voff, u32x4 srd, u32 soff) {
    u32 keep;
    asm volatile("s_mov_b32 %0, m0\n\ts_mov_b32 m0, %1\n\ts_nop 0\n\tbuffer_load_dwordx4 %2, %3, %4 offen lds\n\ts_mov_b32 m0, %0"
                 : "=&s"(keep) : "s"(lds), "v"(voff), "s"(srd), "s"(soff) : "memory");
}
DEVI void dma_lo32(u32 lds, u32 voff, u32x4 srd, u32 soff) {
    u32 keep;
    asm volatile("s_mov_b32 %0, m0\n\ts_mov_b32 m0, %1\n\ts_mov_b32 exec_hi, 0\n\tbuffer_load_dwordx4 %2, %3, %4 offen lds\n\ts_mov_b32 exec_hi, -1\n\ts_mov_b32 m0, %0"
                 : "=&s"(keep) : "s"(lds), "v"(voff), "s"(srd), "s"(soff) : "memory");
}
DEVI void dma_g56(u32 lds, const char* p) {
    u32 keep;
    asm volatile("s_mov_b32 %0, m0\n\ts_mov_b32 m0, %1\n\ts_mov_b32 exec_hi, 0xffffff\n\tglobal_load_lds_dwordx4 %2, off\n\ts_mov_b32 exec_hi, -1\n\ts_mov_b32 m0, %0"
                 : "=&s"(keep) : "s"(lds), "v"(p) : "memory");
}
#define WAIT_BAR(n) asm volatile("s_waitcnt vmcnt(" #n ") lgkmcnt(0)\n\ts_barrier" ::: "memory")
#define WAIT_VM(n) asm volatile("s_waitcnt vmcnt(" #n ")" ::: "memory")
#define BAR() asm volatile("s_waitcnt lgkmcnt(0)\n\ts_barrier" ::: "memory")

#define STAMP(i) do {} while (0)

typedef __amdgpu_buffer_rsrc_t rsrc_t;
DEVI u32x4 ld_sc1(rsrc_t r, u32 voff, u32 soff) { return __builtin_amdgcn_raw_buffer_load_b128(r, voff, soff, 16); }
DEVI void st_sc1(rsrc_t r, u32x4 v, u32 voff, u32 soff) { __builtin_amdgcn_raw_buffer_store_b128(v, r, voff, soff, 16); }

DEVI void wait_flags(rsrc_t r, u32 voff, bool act, u32 tlo, u32 thi) {
    const u64 am = __builtin_amdgcn_ballot_w64(act);
    u32x2 v; u64 sv, ne; u32 cnt;
    asm volatile(
        "s_mov_b64 %[sv], exec\n\ts_mov_b64 exec, %[am]\n\ts_mov_b32 %[cnt], 20000\n"
        "1:\n\tbuffer_load_dwordx2 %[v], %[off], %[r], 0 offen sc1\n\ts_waitcnt vmcnt(0)\n\t"
        "v_cmp_ne_u64 %[ne], %[v], %[tag]\n\ts_cmp_eq_u64 %[ne], 0\n\ts_cbranch_scc1 2f\n\t"
        "s_sleep 1\n\ts_sub_u32 %[cnt], %[cnt], 1\n\ts_cmp_lg_u32 %[cnt], 0\n\ts_cbranch_scc1 1b\n"
        "2:\n\ts_mov_b64 exec, %[sv]"
        : [v] "=&v"(v), [sv] "=&s"(sv), [ne] "=&s"(ne), [cnt] "=&s"(cnt)
        : [off] "v"(voff), [r] "s"(r), [am] "s"(am), [tag] "s"(((u64)thi << 32) | tlo) : "memory", "scc");
}
DEVI void set_flag(rsrc_t r, u32 off, u32 tlo, u32 thi) {
    u32x2 v = {tlo, thi};
    __builtin_amdgcn_raw_buffer_store_b64(v, r, off, 0, 16);
}

template <bool VT>
DEVI void p1_round(u32 sb, u32 xa0, u32 xa1, u32 wa0, u32 wa1, u32 wc, f32x4 (&acc)[6], f32x4& cs, float& s1, float& s2) {
    const u32x4 ones = {0x3f803f80u, 0x3f803f80u, 0x3f803f80u, 0x3f803f80u};
#pragma unroll
    for (int cl = 0; cl < 2; cl++) {
        u32x4 xf = lds128(sb + (cl ? xa1 : xa0));
#pragma unroll
        for (int ct = 0; ct < 6; ct++) {
            u32 o = sb + ((ct & 1) ? wa1 : wa0) + 6144 * cl + 512 * (ct >> 1);
            u32x4 wf = trfrag(o, o + 1536);
            acc[ct] = VT ? mfma(xf, wf, acc[ct]) : mfma(wf, xf, acc[ct]);
        }
        u32 o = sb + wc + 6144 * cl;
        cs = mfma(trfrag(o, o + 1536), ones, cs);
#pragma unroll
        for (int j = 0; j < 4; j++) { s1 = dot2(xf[j], 0x3f803f80u, s1); s2 = dot2(xf[j], xf[j], s2); }
    }
}

template <bool VT>
DEVI void p1_body(int w, int lane, int rot, u32x4 sx, u32x4 sw, u32x4 so, u32 vx0, u32 vx1, u32 vw, u32 vwo, bool p3act,
                  rsrc_t rws, int tt, int col0, bool skip0, float qs) {
    const int i = lane & 15, g = lane >> 4, q = (lane >> 2) & 3, p = lane & 3;
    const u32 xa0 = 2048 * w + 1024 * (i >> 3) + 128 * (i & 7) + 16 * (g ^ ((i >> 1) & 7));
    const u32 xa1 = xa0 ^ 64;
    const u32 wb = 16384 + 3072 * (g >> 1) + 64 * (4 * (g & 1) + q) + 8 * p;
    const u32 wa0 = wb + 32 * (g & 1), wa1 = wb + 32 * ((g & 1) ^ 1);
    const int ctw = w < 6 ? w : w - 6;
    const u32 wc = ((ctw & 1) ? wa1 : wa0) + 512 * (ctw >> 1);
    f32x4 acc[6], cs = {0, 0, 0, 0};
#pragma unroll
    for (int c = 0; c < 6; c++) acc[c] = (f32x4){0, 0, 0, 0};
    float s1 = 0.f, s2 = 0.f;
#define P1_ISSUE(r, slot) do { int kr = (r) + rot; if (kr >= 7) kr -= 7; \
        dma((slot) * L_SLOT + 2048 * w, vx0, sx, kr * 128); dma((slot) * L_SLOT + 2048 * w + 1024, vx1, sx, kr * 128); \
        dma((slot) * L_SLOT + 16384 + 1536 * w, vw, sw, kr * (64 * 2688)); \
        dma_lo32((slot) * L_SLOT + 16384 + 1536 * w + 1024, vw, sw, kr * (64 * 2688) + 128); } while (0)
    P1_ISSUE(0, 0); P1_ISSUE(1, 1); P1_ISSUE(2, 2); P1_ISSUE(3, 3); P1_ISSUE(4, 4);
    WAIT_BAR(16); p1_round<VT>(0 * L_SLOT, xa0, xa1, wa0, wa1, wc, acc, cs, s1, s2);
    WAIT_BAR(12); P1_ISSUE(5, 0); p1_round<VT>(1 * L_SLOT, xa0, xa1, wa0, wa1, wc, acc, cs, s1, s2);
    WAIT_BAR(12); P1_ISSUE(6, 1); p1_round<VT>(2 * L_SLOT, xa0, xa1, wa0, wa1, wc, acc, cs, s1, s2);
    WAIT_BAR(12); p1_round<VT>(3 * L_SLOT, xa0, xa1, wa0, wa1, wc, acc, cs, s1, s2);
    WAIT_BAR(8); p1_round<VT>(4 * L_SLOT, xa0, xa1, wa0, wa1, wc, acc, cs, s1, s2);
    WAIT_BAR(4); p1_round<VT>(0 * L_SLOT, xa0, xa1, wa0, wa1, wc, acc, cs, s1, s2);
    WAIT_BAR(0); p1_round<VT>(1 * L_SLOT, xa0, xa1, wa0, wa1, wc, acc, cs, s1, s2);
#undef P1_ISSUE
    if (w < 6 && i == 0) ldsf4w(L_SCR + 64 * w + 16 * g, cs);
    s1 = xsum(s1); s2 = xsum(s2);
    float mean = s1 * (1.f / 448.f);
    float var = fmaxf(s2 * (1.f / 448.f) - mean * mean, 0.f);
    float rstd = __builtin_amdgcn_rsqf(var + 1e-5f) * qs;
    float mr = -mean * rstd;
    f32x4 rs4, mr4;
    if (VT) {
        if (g == 0) { *(LAS float*)(smem + L_SCR + 512 + 128 * w + 4 * i) = rstd; *(LAS float*)(smem + L_SCR + 512 + 128 * w + 64 + 4 * i) = mr; }
    }
    BAR();
    if (VT) { rs4 = ldsf4(L_SCR + 512 + 128 * w + 16 * g); mr4 = ldsf4(L_SCR + 512 + 128 * w + 64 + 16 * g); }
#pragma unroll
    for (int u = 0; u < 3; u++) {
        u32 pkd[2][2];
#pragma unroll
        for (int t = 0; t < 2; t++) {
            const int ct = 2 * u + t;
            f32x4 v;
            if (VT) { float c = *(LAS float*)(smem + L_SCR + 64 * ct + 4 * i); v = acc[ct] * rs4 + mr4 * c; }
            else { f32x4 c = ldsf4(L_SCR + 64 * ct + 16 * g); v = acc[ct] * rstd + c * mr; }
            pkd[t][0] = pk2(v[0], v[1]); pkd[t][1] = pk2(v[2], v[3]);
        }
        auto a = __builtin_amdgcn_permlane16_swap(pkd[0][0], pkd[1][0], false, false);
        auto b = __builtin_amdgcn_permlane16_swap(pkd[0][1], pkd[1][1], false, false);
        u32x4 o = {a[0], b[0], a[1], b[1]};
        u32 off;
        if (VT) off = WS_V + ((2 * tt + (g >> 1)) * 448 + (col0 - 896) + 32 * u + 16 * (g & 1) + i) * 16;
        else off = WS_QK + ((tt * 112 + (col0 >> 3) + 4 * u + (g >> 1) + 2 * (g & 1)) * 16 + i) * 16;
        if (u > 0 || !skip0) st_sc1(rws, o, off, 0);
    }
}

__global__ void __launch_bounds__(512, 2)
fused(const char* __restrict__ gx, const char* __restrict__ gwqkv, const char* __restrict__ gwo, const char* __restrict__ gwg,
      const char* __restrict__ gwu, const char* __restrict__ gwd, char* gout, char* ws) {
    const int lane = threadIdx.x & 63;
    const int w = __builtin_amdgcn_readfirstlane(threadIdx.x >> 6);
    const int b = blockIdx.x, xcd = b & 7, s = b >> 3;
    const int i = lane & 15, g = lane >> 4, q = (lane >> 2) & 3, p = lane & 3;
    STAMP(0);
    u64 ep = *(const CAS u64*)(ws + WS_EPOCH);
    u64 tg64 = ep + 0x9E3779B97F4A7C15ull;
    tg64 = (tg64 ^ (tg64 >> 30)) * 0xBF58476D1CE4E5B9ull; tg64 = (tg64 ^ (tg64 >> 27)) * 0x94D049BB133111EBull; tg64 ^= tg64 >> 31;
    const u32 tlo = (u32)tg64, thi = (u32)(tg64 >> 32);

    const rsrc_t rws = __builtin_amdgcn_make_buffer_rsrc(ws, 0, WS_END + 65536 * 4, 0x00020000);
    const u32x4 sx = mksrd(gx, 2048 * 896), sw = mksrd(gwqkv, 448 * 2688), so = mksrd(gwo, 448 * 896), sd = mksrd(gwd, 1792 * 896);
    const bool p3act = s < 28;
    const int tb = 4 * xcd + s / 7, nb = s % 7;
    const u32 vwo = (lane >> 3) * 896 + nb * 128 + 16 * ((lane & 7) ^ (2 * ((lane >> 4) & 3)));
    asm volatile("s_nop 4" ::: "memory");

    if (s < 30) {
        const int idx = (xcd & 1) * 30 + s, blk = idx >> 2, T1 = 4 * (xcd >> 1) + (idx & 3);
        const int mat = blk / 5, j5 = blk % 5;
        const int col0 = 448 * mat + (j5 == 4 ? 352 : 96 * j5);
        const int rot = (blk + 2 * (idx & 3)) % 7;
        const int xr = lane >> 3;
        const u32 vx0 = (128 * T1 + 16 * w + xr) * 896 + 16 * ((lane & 7) ^ (xr >> 1));
        const u32 vx1 = (vx0 ^ 64) + 8 * 896;
        const int sl = (lane >> 2) & 7;
        const u32 vw = (32 * (w >> 2) + 16 * ((w >> 1) & 1) + 8 * (sl >> 2) + 4 * (w & 1) + (sl & 3)) * 2688 + col0 * 2 + 64 * (lane >> 5) + 16 * ((lane & 3) ^ (2 * (sl >> 2)));
        const float qs = mat == 0 ? 0.18898223650461362f * 1.4426950408889634f : 1.f;
        if (mat == 2) p1_body<true>(w, lane, rot, sx, sw, so, vx0, vx1, vw, vwo, p3act, rws, 8 * T1 + w, col0, j5 == 4, qs);
        else p1_body<false>(w, lane, rot, sx, sw, so, vx0, vx1, vw, vwo, p3act, rws, 8 * T1 + w, col0, j5 == 4, qs);
        STAMP(1);
        WAIT_BAR(0);
        if (threadIdx.x == 0) set_flag(rws, WS_FA + (T1 * 16 + blk) * 8, tlo, thi);
    }
    STAMP(2);
    const int tg = 2 * (xcd >> 1) + (s >> 4), fs = 16 * (xcd & 1) + (s & 15);
    const int gl = lane < 56 ? lane : 55;
    const char* gup = ((gl % 14) < 7 ? gwg + (gl % 14) * 16 : gwu + (gl % 14 - 7) * 16) + (4 * w + gl / 14) * 3584 + fs * 112;
    if (p3act) {
#pragma unroll
        for (int k = 0; k < 7; k++) dma(L_WO + (7 * w + k) * 1024, vwo, so, (7 * w + k) * 8 * 896);
    }
#pragma unroll
    for (int a = 8; a < 14; a++) dma_g56(L_GUHI + (32 * (a - 8) + 4 * w) * 224, gup + a * (32 * 3584));

    {
        const int T = 4 * (xcd >> 1) + (s >> 3), h = 8 * (xcd & 1) + (s & 7);
        const int c0 = (7 * h) >> 1;
        {
            int Tp = T - 2 + lane / 15;
            wait_flags(rws, WS_FA + (Tp * 16 + lane % 15) * 8, lane < 45 && Tp >= 0, tlo, thi);
        }
        STAMP(3);
        const bool fg = (h & 1) ? (g == 0) : (g == 3);
        u32x4 qf = ld_sc1(rws, WS_QK + (((8 * T + w) * 112 + c0 + g) * 16 + i) * 16, 0);
        u32x4 kv[6];
#pragma unroll
        for (int k = 0; k < 3; k++) {
            const int n = 3 * w + k;
            const int tt = 8 * T - 16 + n;
            kv[k] = (u32x4){0, 0, 0, 0};
            if (tt >= 0) kv[k] = ld_sc1(rws, WS_QK + ((56 + c0 + g) * 16 + i) * 16, tt * (112 * 256));
            const int G = 4 * T - 8 + (n >> 1);
            int F = 16 * (n & 1) + i; F = F > 27 ? 27 : F;
            kv[3 + k] = (u32x4){0, 0, 0, 0};
            if (G >= 0) kv[3 + k] = ld_sc1(rws, WS_V + (g * 448 + 28 * h + F) * 16, G * (4 * 448 * 16));
        }
#pragma unroll
        for (int k = 0; k < 3; k++) {
            const int n = 3 * w + k;
            const int tt = 8 * T - 16 + n;
            u32x4 kf = kv[k];
            const u32 padv = tt >= 0 ? 0u : 0xF149u;
            if (h & 1) { kf[0] = fg ? padv : kf[0]; kf[1] = fg ? 0u : kf[1]; } else { kf[2] = fg ? padv : kf[2]; kf[3] = fg ? 0u : kf[3]; }
            lds128w(L_K + (2 * (n >> 1) + ((i >> 2) & 1)) * 1024 + (16 * g + 8 * (n & 1) + 4 * (i >> 3) + (i & 3)) * 16, kf);
            u32x4 vf = kv[3 + k];
            if (n & 1) {
                const u32 o1 = i == 12 ? 0x3f803f80u : 0u;
#pragma unroll
                for (int j = 0; j < 4; j++) vf[j] = i >= 12 ? o1 : vf[j];
            }
            lds128w(L_V + n * 1024 + lane * 16, vf);
        }
        if (h & 1) { qf[0] = fg ? 0x3f80u : qf[0]; qf[1] = fg ? 0u : qf[1]; } else { qf[2] = fg ? 0x3f80u : qf[2]; qf[3] = fg ? 0u : qf[3]; }
        BAR();
        STAMP(4);
        f32x4 sc[18];
        const int th = 16 * (w & 1) + i - 8 * g;
        const u32 kb = L_K + (w >> 1) * 2048 + lane * 16;
#pragma unroll
        for (int n = 0; n < 18; n++) {
            f32x4 cin = {0, 0, 0, 0};
            if (n < 2 || n >= 16) {
#pragma unroll
                for (int r = 0; r < 4; r++) { bool le = 4 * (n & 1) + r <= th; cin[r] = (n < 2 ? le : !le) ? -1e30f : 0.f; }
            }
            sc[n] = mfma(lds128(kb + n * 1024), qf, cin);
        }
        float m = -3e38f;
#pragma unroll
        for (int n = 0; n < 18; n++) { m = fmaxf(fmaxf(m, sc[n][0]), sc[n][1]); m = fmaxf(fmaxf(m, sc[n][2]), sc[n][3]); }
        m = xmax(m);
        f32x4 o0 = {0, 0, 0, 0}, o1 = {0, 0, 0, 0};
        const u32 vb = L_V + (w >> 1) * 2048 + lane * 16;
#pragma unroll
        for (int gi = 0; gi < 9; gi++) {
            f32x4 a = sc[2 * gi] - m, c = sc[2 * gi + 1] - m;
            u32x4 pb = {pk2(__builtin_amdgcn_exp2f(a[0]), __builtin_amdgcn_exp2f(a[1])), pk2(__builtin_amdgcn_exp2f(a[2]), __builtin_amdgcn_exp2f(a[3])),
                        pk2(__builtin_amdgcn_exp2f(c[0]), __builtin_amdgcn_exp2f(c[1])), pk2(__builtin_amdgcn_exp2f(c[2]), __builtin_amdgcn_exp2f(c[3]))};
            o0 = mfma(lds128(vb + gi * 2048), pb, o0);
            o1 = mfma(lds128(vb + gi * 2048 + 1024), pb, o1);
        }
        float l = xsum(g == 3 ? o1[0] : 0.f);
        float rl = __builtin_amdgcn_rcpf(l);
        o0 *= rl; o1 *= rl;
        u32x4 of = {pk2(o0[0], o0[1]), pk2(o0[2], o0[3]), g == 3 ? 0u : pk2(o1[0], o1[1]), g == 3 ? 0u : pk2(o1[2], o1[3])};
        st_sc1(rws, of, WS_O + lane * 16, ((8 * T + w) * 16 + h) * 1024);
        STAMP(5);
        WAIT_BAR(0);
        if (threadIdx.x == 0) set_flag(rws, WS_FB + (T * 16 + h) * 8, tlo, thi);
    }
#pragma unroll
    for (int a = 0; a < 8; a++) dma_g56((32 * a + 4 * w) * 224, gup + a * (32 * 3584));
    STAMP(6);

    u32x4 hres = {0, 0, 0, 0};
    const int ttl = w & 3, kh = w >> 2;
    if (p3act) {
        const rsrc_t rx = __builtin_amdgcn_make_buffer_rsrc((void*)gx, 0, 2048 * 896, 0x00020000);
        const u32 xo = (64 * tb + 16 * ttl + i) * 896 + (64 * nb + 32 * kh + 4 * g) * 2;
        u32x2 xr0 = __builtin_amdgcn_raw_buffer_load_b64(rx, xo, 0, 0), xr1 = __builtin_amdgcn_raw_buffer_load_b64(rx, xo + 32, 0, 0);
        u32x4 wf[8][4];
        const int g1 = g == 3 ? 1 : g;
        const u32 fe = 2 * (g & 1) + (q >> 1);
        const u32 r0 = L_WO + (224 * kh + 4 * g + q) * 128 + 8 * p, r1 = L_WO + (224 * kh + 16 + 4 * g1 + q) * 128 + 8 * p;
#pragma unroll
        for (int hh = 0; hh < 8; hh++)
#pragma unroll
            for (int t = 0; t < 4; t++) {
                const u32 sw32 = 32 * ((t ^ (2 * (hh & 1))) ^ fe);
                wf[hh][t] = trfrag(r0 + sw32 + hh * 28 * 128, r1 + sw32 + hh * 28 * 128);
            }
        BAR();
        wait_flags(rws, WS_FB + ((tb >> 1) * 16 + i) * 8, lane < 16, tlo, thi);
        STAMP(7);
        u32x4 of[8];
#pragma unroll
        for (int hh = 0; hh < 8; hh++) of[hh] = ld_sc1(rws, WS_O + lane * 16, ((4 * tb + ttl) * 16 + 8 * kh + hh) * 1024);
        f32x4 acc[4];
#pragma unroll
        for (int t = 0; t < 4; t++) acc[t] = (f32x4){0, 0, 0, 0};
#pragma unroll
        for (int hh = 0; hh < 8; hh++)
#pragma unroll
            for (int t = 0; t < 4; t++) acc[t] = mfma(wf[hh][t], of[hh], acc[t]);
        ldsf4w(L_WO + (w ^ 4) * 2048 + lane * 16, kh ? acc[0] : acc[2]);
        ldsf4w(L_WO + (w ^ 4) * 2048 + 1024 + lane * 16, kh ? acc[1] : acc[3]);
        BAR();
        f32x4 h0 = (kh ? acc[2] : acc[0]) + ldsf4(L_WO + w * 2048 + lane * 16);
        f32x4 h1 = (kh ? acc[3] : acc[1]) + ldsf4(L_WO + w * 2048 + 1024 + lane * 16);
        hres[0] = pk2(h0[0] + bflo(xr0[0]), h0[1] + bfhi(xr0[0])); hres[1] = pk2(h0[2] + bflo(xr0[1]), h0[3] + bfhi(xr0[1]));
        hres[2] = pk2(h1[0] + bflo(xr1[0]), h1[1] + bfhi(xr1[0])); hres[3] = pk2(h1[2] + bflo(xr1[1]), h1[3] + bfhi(xr1[1]));
        st_sc1(rws, hres, WS_H + lane * 16, ((4 * tb + ttl) * 14 + 2 * nb + kh) * 1024);
        STAMP(8);
        WAIT_BAR(0);
        if (threadIdx.x == 0) set_flag(rws, WS_FC + (tb * 8 + nb) * 8, tlo, thi);
    } else {
        WAIT_BAR(0);
    }

    {
        const u32 gb = (4 * g + q) * 224 + 8 * p;
        const u32 mb = (4 * g + q) * 224 + (p < 2 ? 96 : 192) + 8 * p;
        {
            const u32x4 ones = {0x3f803f80u, 0x3f803f80u, 0x3f803f80u, 0x3f803f80u};
            const u32 cb = w >= 6 ? mb : gb + (w < 3 ? 32 * w : 112 + 32 * (w - 3));
            f32x4 cs = {0, 0, 0, 0};
#pragma unroll
            for (int c = 0; c < 14; c++) { const u32 ro = (c < 8 ? 0 : L_GUHI - 57344) + c * 7168; cs = mfma(trfrag(cb + ro, cb + ro + 3584), ones, cs); }
            if (w < 7 && i == 0) ldsf4w(L_SCR + 64 * w + 16 * g, cs);
            BAR();
        }
        wait_flags(rws, WS_FC + ((4 * tg + (w >> 1)) * 8 + i) * 8, lane < 7, tlo, thi);
        STAMP(9);
        const int tt0 = 16 * tg + 2 * w;
        u32x4 hf[2][14];
#pragma unroll
        for (int c = 0; c < 14; c++)
#pragma unroll
            for (int t = 0; t < 2; t++) hf[t][c] = ld_sc1(rws, WS_H + lane * 16, ((tt0 + t) * 14 + c) * 1024);
        f32x4 acc[2][7];
#pragma unroll
        for (int t = 0; t < 2; t++)
#pragma unroll
            for (int n = 0; n < 7; n++) acc[t][n] = (f32x4){0, 0, 0, 0};
        float s1[2] = {0.f, 0.f}, s2[2] = {0.f, 0.f};
#pragma unroll
        for (int c = 0; c < 14; c++) {
            const u32 ro = (c < 8 ? 0 : L_GUHI - 57344) + c * 7168;
#pragma unroll
            for (int n = 0; n < 7; n++) {
                const u32 a = (n == 6 ? mb : gb + (n < 3 ? 32 * n : 112 + 32 * (n - 3))) + ro;
                u32x4 wf = trfrag(a, a + 3584);
                acc[0][n] = mfma(wf, hf[0][c], acc[0][n]);
                acc[1][n] = mfma(wf, hf[1][c], acc[1][n]);
            }
#pragma unroll
            for (int t = 0; t < 2; t++)
#pragma unroll
                for (int j = 0; j < 4; j++) { s1[t] = dot2(hf[t][c][j], 0x3f803f80u, s1[t]); s2[t] = dot2(hf[t][c][j], hf[t][c][j], s2[t]); }
        }
        STAMP(10);
        u32 pa[2][3][2]; f32x4 mm[2];
#pragma unroll
        for (int t = 0; t < 2; t++) {
            float a1 = xsum(s1[t]), a2 = xsum(s2[t]);
            float mean = a1 * (1.f / 448.f);
            float var = fmaxf(a2 * (1.f / 448.f) - mean * mean, 0.f);
            float rstd = __builtin_amdgcn_rsqf(var + 1e-5f), mr = -mean * rstd;
#pragma unroll
            for (int n = 0; n < 3; n++) {
                f32x4 gt = acc[t][n] * rstd + ldsf4(L_SCR + 64 * n + 16 * g) * mr;
                f32x4 up = acc[t][3 + n] * rstd + ldsf4(L_SCR + 64 * (3 + n) + 16 * g) * mr;
                f32x4 av;
#pragma unroll
                for (int r = 0; r < 4; r++) av[r] = gt[r] * up[r] * __builtin_amdgcn_rcpf(1.f + __builtin_amdgcn_exp2f(gt[r] * -1.4426950408889634f));
                pa[t][n][0] = pk2(av[0], av[1]); pa[t][n][1] = pk2(av[2], av[3]);
            }
            mm[t] = acc[t][6] * rstd + ldsf4(L_SCR + 64 * 6 + 16 * g) * mr;
        }
        f32x4 av;
#pragma unroll
        for (int r = 0; r < 4; r++) {
            auto sw2 = __builtin_amdgcn_permlane32_swap(asu(mm[0][r]), asu(mm[1][r]), false, false);
            float gt = asf(sw2[0]), up = asf(sw2[1]);
            av[r] = gt * up * __builtin_amdgcn_rcpf(1.f + __builtin_amdgcn_exp2f(gt * -1.4426950408889634f));
        }
        const u32 m0 = pk2(av[0], av[1]), m1 = pk2(av[2], av[3]);
        const int a4 = fs >> 2, pp = fs & 3;
#pragma unroll
        for (int t = 0; t < 2; t++) {
            u32x4 v = {pa[t][0][0], pa[t][0][1], pa[t][1][0], pa[t][1][1]};
            st_sc1(rws, v, WS_ACT + lane * 16, ((tt0 + t) * 56 + 7 * a4 + pp) * 1024);
        }
        {
            auto x0 = __builtin_amdgcn_permlane32_swap(pa[0][2][0], pa[1][2][0], false, false);
            auto x1 = __builtin_amdgcn_permlane32_swap(pa[0][2][1], pa[1][2][1], false, false);
            u32x4 v = {x0[0], x1[0], x0[1], x1[1]};
            st_sc1(rws, v, WS_ACT + (g >> 1) * (56 * 1024) + ((g & 1) * 16 + i) * 16, (tt0 * 56 + 7 * a4 + 4 + (pp >> 1)) * 1024 + (pp & 1) * 512);
        }
        {
            auto y0 = __builtin_amdgcn_permlane16_swap(m0, m0, false, false);
            auto y1 = __builtin_amdgcn_permlane16_swap(m1, m1, false, false);
            u32x4 v = {y0[0], y1[0], y0[1], y1[1]};
            if ((g & 1) == 0) st_sc1(rws, v, WS_ACT + (g >> 1) * (56 * 1024) + i * 16, (tt0 * 56 + 7 * a4 + 6) * 1024 + pp * 256);
        }
        STAMP(11);
        WAIT_BAR(0);
        if (threadIdx.x == 0) set_flag(rws, WS_FD + (tg * 32 + fs) * 8, tlo, thi);
    }

    if (p3act) {
        u32x4 wd[7][4];
        const u32 wb = 16384 * w;
        const u32 fe = 2 * (g & 1) + (q >> 1);
        const u32 lb = wb + (4 * (g & 1) + q) * 128 + 8 * p;
#pragma unroll
        for (int half = 0; half < 2; half++) {
#pragma unroll
            for (int k = 0; k < 14; k++) {
                const int pl = k / 7, pc = k % 7;
                const u32 dst = pc < 6 ? (pl * 6 + pc) * 1024 : (12 + 2 * half + pl) * 1024;
                dma(wb + dst, vwo, sd, (224 * w + 112 * half + 8 * k) * 896);
            }
            WAIT_VM(0);
#pragma unroll
            for (int t = 0; t < 4; t++) {
                const u32 a = lb + 32 * (t ^ fe);
#pragma unroll
                for (int pl = 0; pl < 2; pl++) wd[2 * half + pl][t] = trfrag(a + (pl * 6 + (g >> 1)) * 1024, a + (pl * 6 + 2 + (g >> 1)) * 1024);
                wd[4 + half][t] = trfrag(a + ((g >> 1) * 6 + 4) * 1024, a + ((g >> 1) * 6 + 5) * 1024);
            }
            asm volatile("s_waitcnt lgkmcnt(0)" ::: "memory");
        }
#pragma unroll
        for (int t = 0; t < 4; t++) {
            const u32 a = wb + (12 + g) * 1024 + q * 128 + 8 * p;
            wd[6][t] = trfrag(a + 32 * (t ^ (q >> 1)), a + 512 + 32 * (t ^ (2 + (q >> 1))));
        }
        BAR();
        STAMP(12);
        wait_flags(rws, WS_FD + ((tb >> 2) * 32 + 4 * w + i) * 8, lane < 4, tlo, thi);
        STAMP(13);
#pragma unroll
        for (int t2 = 0; t2 < 4; t2++) {
            u32x4 af[7];
#pragma unroll
            for (int k = 0; k < 7; k++) af[k] = ld_sc1(rws, WS_ACT + lane * 16, ((4 * tb + t2) * 56 + 7 * w + k) * 1024);
            f32x4 acc[4];
#pragma unroll
            for (int t = 0; t < 4; t++) acc[t] = (f32x4){0, 0, 0, 0};
#pragma unroll
            for (int k = 0; k < 7; k++)
#pragma unroll
                for (int t = 0; t < 4; t++) acc[t] = mfma(wd[k][t], af[k], acc[t]);
#pragma unroll
            for (int t = 0; t < 4; t++) ldsf4w(((t2 * 4 + t) * 8 + w) * 1024 + lane * 16, acc[t]);
        }
        BAR();
        f32x4 r0 = {0, 0, 0, 0}, r1 = {0, 0, 0, 0};
#pragma unroll
        for (int k = 0; k < 8; k++) {
            r0 += ldsf4(((ttl * 4 + 2 * kh) * 8 + k) * 1024 + lane * 16);
            r1 += ldsf4(((ttl * 4 + 2 * kh + 1) * 8 + k) * 1024 + lane * 16);
        }
        u32 a0 = pk2(r0[0] + bflo(hres[0]), r0[1] + bfhi(hres[0])), a1 = pk2(r0[2] + bflo(hres[1]), r0[3] + bfhi(hres[1]));
        u32 b0 = pk2(r1[0] + bflo(hres[2]), r1[1] + bfhi(hres[2])), b1 = pk2(r1[2] + bflo(hres[3]), r1[3] + bfhi(hres[3]));
        auto x0 = __builtin_amdgcn_permlane16_swap(a0, b0, false, false);
        auto x1 = __builtin_amdgcn_permlane16_swap(a1, b1, false, false);
        u32x4 v = {x0[0], x1[0], x0[1], x1[1]};
        *(u32x4*)(gout + (size_t)(64 * tb + 16 * ttl + i) * 896 + (64 * nb + 32 * kh + 8 * (g >> 1) + 16 * (g & 1)) * 2) = v;
        STAMP(14);
    } else if (b == 255 && w == 0) {
#pragma unroll
        for (int k = 0; k < 4; k++) wait_flags(rws, WS_FD + (k * 64 + lane) * 8, true, tlo, thi);
        if (lane == 0) set_flag(rws, WS_EPOCH, tlo, thi);
    }
}

extern "C" void kernel_launch(void* const* d_in, const int* in_sizes, int n_in, void* d_out, int out_size, void* d_ws, size_t ws_size,
                              hipStream_t stream) {
    fused<<<256, 512, 0, stream>>>((const char*)d_in[0], (const char*)d_in[1], (const char*)d_in[2], (const char*)d_in[4],
                                   (const char*)d_in[5], (const char*)d_in[6], (char*)d_out, (char*)d_ws);
}
```

```cpp
#define STAMPS
#define SEL 15
#define AMP 0
#define MODE 0
#include <hip/hip_runtime.h>
#include <stdint.h>

typedef unsigned u32;
typedef unsigned long long u64;
typedef u32 u32x2 __attribute__((ext_vector_type(2)));
typedef u32 u32x4 __attribute__((ext_vector_type(4)));
typedef float f32x2 __attribute__((ext_vector_type(2)));
typedef float f32x4 __attribute__((ext_vector_type(4)));
typedef __bf16 bf16x2 __attribute__((ext_vector_type(2)));
typedef __bf16 bf16x8 __attribute__((ext_vector_type(8)));
typedef short s16x4 __attribute__((ext_vector_type(4)));
typedef short s16x8 __attribute__((ext_vector_type(8)));
#define LAS __attribute__((address_space(3)))
#define CAS __attribute__((address_space(4)))
#define DEVI __device__ __forceinline__

constexpr u32 WS_EPOCH = 0;
constexpr u32 WS_FA = 4096;
constexpr u32 WS_FB = WS_FA + 2048;
constexpr u32 WS_FC = WS_FB + 2048;
constexpr u32 WS_FD = WS_FC + 2048;
constexpr u32 WS_QK = 65536;
constexpr u32 WS_V = WS_QK + 128 * 112 * 256;
constexpr u32 WS_O = WS_V + 256 * 448 * 16;
constexpr u32 WS_H = WS_O + 128 * 16 * 1024;
constexpr u32 WS_ACT = WS_H + 128 * 14 * 1024;
constexpr u32 WS_END = WS_ACT + 128 * 56 * 1024;
constexpr u32 WS_STAMP = WS_END;

constexpr u32 L_SLOT = 28672;
constexpr u32 L_WO = 57344;
constexpr u32 L_GUHI = 114688;
constexpr u32 L_SCR = 157696;
constexpr u32 L_K = 0, L_V = 24576;

__shared__ __attribute__((aligned(1024))) char smem[163840];

DEVI f32x4 mfma(u32x4 a, u32x4 b, f32x4 c) {
    return __builtin_amdgcn_mfma_f32_16x16x32_bf16(__builtin_bit_cast(bf16x8, a), __builtin_bit_cast(bf16x8, b), c, 0, 0, 0);
}
DEVI u32x4 trfrag(u32 a0, u32 a1) {
    s16x4 lo = __builtin_amdgcn_ds_read_tr16_b64_v4i16((LAS s16x4*)(smem + a0));
    s16x4 hi = __builtin_amdgcn_ds_read_tr16_b64_v4i16((LAS s16x4*)(smem + a1));
    return __builtin_bit_cast(u32x4, __builtin_shufflevector(lo, hi, 0, 1, 2, 3, 4, 5, 6, 7));
}
DEVI u32x4 lds128(u32 a) { return *(LAS u32x4*)(smem + a); }
DEVI void lds128w(u32 a, u32x4 v) { *(LAS u32x4*)(smem + a) = v; }
DEVI f32x4 ldsf4(u32 a) { return *(LAS f32x4*)(smem + a); }
DEVI void ldsf4w(u32 a, f32x4 v) { *(LAS f32x4*)(smem + a) = v; }
DEVI u32 pk2(float a, float b) { f32x2 f = {a, b}; return __builtin_bit_cast(u32, __builtin_convertvector(f, bf16x2)); }
DEVI float dot2(u32 a, u32 b, float c) { return __builtin_amdgcn_fdot2_f32_bf16(__builtin_bit_cast(bf16x2, a), __builtin_bit_cast(bf16x2, b), c, false); }
DEVI float bflo(u32 v) { return __builtin_bit_cast(float, v << 16); }
DEVI float bfhi(u32 v) { return __builtin_bit_cast(float, v & 0xffff0000u); }
DEVI float asf(u32 v) { return __builtin_bit_cast(float, v); }
DEVI u32 asu(float v) { return __builtin_bit_cast(u32, v); }
DEVI float xsum(float v) {
    u32 u = __builtin_bit_cast(u32, v);
    auto s = __builtin_amdgcn_permlane16_swap(u, u, false, false);
    v = asf(s[0]) + asf(s[1]);
    u = __builtin_bit_cast(u32, v);
    auto t = __builtin_amdgcn_permlane32_swap(u, u, false, false);
    return asf(t[0]) + asf(t[1]);
}
DEVI float xmax(float v) {
    u32 u = __builtin_bit_cast(u32, v);
    auto s = __builtin_amdgcn_permlane16_swap(u, u, false, false);
    v = fmaxf(asf(s[0]), asf(s[1]));
    u = __builtin_bit_cast(u32, v);
    auto t = __builtin_amdgcn_permlane32_swap(u, u, false, false);
    return fmaxf(asf(t[0]), asf(t[1]));
}
DEVI u32x4 mksrd(const void* p, u32 bytes) {
    u64 a = (u64)p;
    u32x4 r = {(u32)a, (u32)(a >> 32) & 0xffffu, bytes, 0x00020000u};
    return r;
}
DEVI void dma(u32 lds, u32 voff, u32x4 srd, u32 soff) {
    u32 keep;
    asm volatile("s_mov_b32 %0, m0\n\ts_mov_b32 m0, %1\n\ts_nop 0\n\tbuffer_load_dwordx4 %2, %3, %4 offen lds\n\ts_mov_b32 m0, %0"
                 : "=&s"(keep) : "s"(lds), "v"(voff), "s"(srd), "s"(soff) : "memory");
}
DEVI void dma_lo32(u32 lds, u32 voff, u32x4 srd, u32 soff) {
    u32 keep;
    asm volatile("s_mov_b32 %0, m0\n\ts_mov_b32 m0, %1\n\ts_mov_b32 exec_hi, 0\n\tbuffer_load_dwordx4 %2, %3, %4 offen lds\n\ts_mov_b32 exec_hi, -1\n\ts_mov_b32 m0, %0"
                 : "=&s"(keep) : "s"(lds), "v"(voff), "s"(srd), "s"(soff) : "memory");
}
DEVI void dma_g56(u32 lds, const char* p) {
    u32 keep;
    asm volatile("s_mov_b32 %0, m0\n\ts_mov_b32 m0, %1\n\ts_mov_b32 exec_hi, 0xffffff\n\tglobal_load_lds_dwordx4 %2, off\n\ts_mov_b32 exec_hi, -1\n\ts_mov_b32 m0, %0"
                 : "=&s"(keep) : "s"(lds), "v"(p) : "memory");
}
#define WAIT_BAR(n) asm volatile("s_waitcnt vmcnt(" #n ") lgkmcnt(0)\n\ts_barrier" ::: "memory")
#define WAIT_VM(n) asm volatile("s_waitcnt vmcnt(" #n ")" ::: "memory")
#define BAR() asm volatile("s_waitcnt lgkmcnt(0)\n\ts_barrier" ::: "memory")

#define STAMP(i) do { st[i] = (u32)__builtin_amdgcn_s_memrealtime(); } while (0)

typedef __amdgpu_buffer_rsrc_t rsrc_t;
DEVI u32x4 ld_sc1(rsrc_t r, u32 voff, u32 soff) { return __builtin_amdgcn_raw_buffer_load_b128(r, voff, soff, 16); }
DEVI void st_sc1(rsrc_t r, u32x4 v, u32 voff, u32 soff) { __builtin_amdgcn_raw_buffer_store_b128(v, r, voff, soff, 16); }

DEVI void wait_flags(rsrc_t r, u32 voff, bool act, u32 tlo, u32 thi) {
    const u64 am = __builtin_amdgcn_ballot_w64(act);
    u32x2 v; u64 sv, ne; u32 cnt;
    asm volatile(
        "s_mov_b64 %[sv], exec\n\ts_mov_b64 exec, %[am]\n\ts_mov_b32 %[cnt], 20000\n"
        "1:\n\tbuffer_load_dwordx2 %[v], %[off], %[r], 0 offen sc1\n\ts_waitcnt vmcnt(0)\n\t"
        "v_cmp_ne_u64 %[ne], %[v], %[tag]\n\ts_cmp_eq_u64 %[ne], 0\n\ts_cbranch_scc1 2f\n\t"
        "s_sleep 1\n\ts_sub_u32 %[cnt], %[cnt], 1\n\ts_cmp_lg_u32 %[cnt], 0\n\ts_cbranch_scc1 1b\n"
        "2:\n\ts_mov_b64 exec, %[sv]"
        : [v] "=&v"(v), [sv] "=&s"(sv), [ne] "=&s"(ne), [cnt] "=&s"(cnt)
        : [off] "v"(voff), [r] "s"(r), [am] "s"(am), [tag] "s"(((u64)thi << 32) | tlo) : "memory", "scc");
}
DEVI void set_flag(rsrc_t r, u32 off, u32 tlo, u32 thi) {
    u32x2 v = {tlo, thi};
    __builtin_amdgcn_raw_buffer_store_b64(v, r, off, 0, 16);
}

template <bool VT>
DEVI void p1_round(u32 sb, u32 xa0, u32 xa1, u32 wa0, u32 wa1, u32 wc, f32x4 (&acc)[6], f32x4& cs, float& s1, float& s2) {
    const u32x4 ones = {0x3f803f80u, 0x3f803f80u, 0x3f803f80u, 0x3f803f80u};
#pragma unroll
    for (int cl = 0; cl < 2; cl++) {
        u32x4 xf = lds128(sb + (cl ? xa1 : xa0));
#pragma unroll
        for (int ct = 0; ct < 6; ct++) {
            u32 o = sb + ((ct & 1) ? wa1 : wa0) + 6144 * cl + 512 * (ct >> 1);
            u32x4 wf = trfrag(o, o + 1536);
            acc[ct] = VT ? mfma(xf, wf, acc[ct]) : mfma(wf, xf, acc[ct]);
        }
        u32 o = sb + wc + 6144 * cl;
        cs = mfma(trfrag(o, o + 1536), ones, cs);
#pragma unroll
        for (int j = 0; j < 4; j++) { s1 = dot2(xf[j], 0x3f803f80u, s1); s2 = dot2(xf[j], xf[j], s2); }
    }
}

template <bool VT>
DEVI void p1_body(int w, int lane, int rot, u32x4 sx, u32x4 sw, u32x4 so, u32 vx0, u32 vx1, u32 vw, u32 vwo, bool p3act,
                  rsrc_t rws, int tt, int col0, bool skip0, float qs) {
    const int i = lane & 15, g = lane >> 4, q = (lane >> 2) & 3, p = lane & 3;
    const u32 xa0 = 2048 * w + 1024 * (i >> 3) + 128 * (i & 7) + 16 * (g ^ ((i >> 1) & 7));
    const u32 xa1 = xa0 ^ 64;
    const u32 wb = 16384 + 3072 * (g >> 1) + 64 * (4 * (g & 1) + q) + 8 * p;
    const u32 wa0 = wb + 32 * (g & 1), wa1 = wb + 32 * ((g & 1) ^ 1);
    const int ctw = w < 6 ? w : w - 6;
    const u32 wc = ((ctw & 1) ? wa1 : wa0) + 512 * (ctw >> 1);
    f32x4 acc[6], cs = {0, 0, 0, 0};
#pragma unroll
    for (int c = 0; c < 6; c++) acc[c] = (f32x4){0, 0, 0, 0};
    float s1 = 0.f, s2 = 0.f;
#define P1_ISSUE(r, slot) do { int kr = (r) + rot; if (kr >= 7) kr -= 7; \
        dma((slot) * L_SLOT + 2048 * w, vx0, sx, kr * 128); dma((slot) * L_SLOT + 2048 * w + 1024, vx1, sx, kr * 128); \
        dma((slot) * L_SLOT + 16384 + 1536 * w, vw, sw, kr * (64 * 2688)); \
        dma_lo32((slot) * L_SLOT + 16384 + 1536 * w + 1024, vw, sw, kr * (64 * 2688) + 128); } while (0)
    P1_ISSUE(0, 0); P1_ISSUE(1, 1); P1_ISSUE(2, 2); P1_ISSUE(3, 3); P1_ISSUE(4, 4);
    WAIT_BAR(16); p1_round<VT>(0 * L_SLOT, xa0, xa1, wa0, wa1, wc, acc, cs, s1, s2);
    WAIT_BAR(12); P1_ISSUE(5, 0); p1_round<VT>(1 * L_SLOT, xa0, xa1, wa0, wa1, wc, acc, cs, s1, s2);
    WAIT_BAR(12); P1_ISSUE(6, 1); p1_round<VT>(2 * L_SLOT, xa0, xa1, wa0, wa1, wc, acc, cs, s1, s2);
    WAIT_BAR(12); p1_round<VT>(3 * L_SLOT, xa0, xa1, wa0, wa1, wc, acc, cs, s1, s2);
    WAIT_BAR(8); p1_round<VT>(4 * L_SLOT, xa0, xa1, wa0, wa1, wc, acc, cs, s1, s2);
    WAIT_BAR(4); p1_round<VT>(0 * L_SLOT, xa0, xa1, wa0, wa1, wc, acc, cs, s1, s2);
    WAIT_BAR(0); p1_round<VT>(1 * L_SLOT, xa0, xa1, wa0, wa1, wc, acc, cs, s1, s2);
#undef P1_ISSUE
    if (w < 6 && i == 0) ldsf4w(L_SCR + 64 * w + 16 * g, cs);
    s1 = xsum(s1); s2 = xsum(s2);
    float mean = s1 * (1.f / 448.f);
    float var = fmaxf(s2 * (1.f / 448.f) - mean * mean, 0.f);
    float rstd = __builtin_amdgcn_rsqf(var + 1e-5f) * qs;
    float mr = -mean * rstd;
    f32x4 rs4, mr4;
    if (VT) {
        if (g == 0) { *(LAS float*)(smem + L_SCR + 512 + 128 * w + 4 * i) = rstd; *(LAS float*)(smem + L_SCR + 512 + 128 * w + 64 + 4 * i) = mr; }
    }
    BAR();
    if (VT) { rs4 = ldsf4(L_SCR + 512 + 128 * w + 16 * g); mr4 = ldsf4(L_SCR + 512 + 128 * w + 64 + 16 * g); }
#pragma unroll
    for (int u = 0; u < 3; u++) {
        u32 pkd[2][2];
#pragma unroll
        for (int t = 0; t < 2; t++) {
            const int ct = 2 * u + t;
            f32x4 v;
            if (VT) { float c = *(LAS float*)(smem + L_SCR + 64 * ct + 4 * i); v = acc[ct] * rs4 + mr4 * c; }
            else { f32x4 c = ldsf4(L_SCR + 64 * ct + 16 * g); v = acc[ct] * rstd + c * mr; }
            pkd[t][0] = pk2(v[0], v[1]); pkd[t][1] = pk2(v[2], v[3]);
        }
        auto a = __builtin_amdgcn_permlane16_swap(pkd[0][0], pkd[1][0], false, false);
        auto b = __builtin_amdgcn_permlane16_swap(pkd[0][1], pkd[1][1], false, false);
        u32x4 o = {a[0], b[0], a[1], b[1]};
        u32 off;
        if (VT) off = WS_V + ((2 * tt + (g >> 1)) * 448 + (col0 - 896) + 32 * u + 16 * (g & 1) + i) * 16;
        else off = WS_QK + ((tt * 112 + (col0 >> 3) + 4 * u + (g >> 1) + 2 * (g & 1)) * 16 + i) * 16;
        if (u > 0 || !skip0) st_sc1(rws, o, off, 0);
    }
}

__global__ void __launch_bounds__(512, 2)
fused(const char* __restrict__ gx, const char* __restrict__ gwqkv, const char* __restrict__ gwo, const char* __restrict__ gwg,
      const char* __restrict__ gwu, const char* __restrict__ gwd, char* gout, char* ws) {
    const int lane = threadIdx.x & 63;
    const int w = __builtin_amdgcn_readfirstlane(threadIdx.x >> 6);
    const int b = blockIdx.x, xcd = b & 7, s = b >> 3;
    const int i = lane & 15, g = lane >> 4, q = (lane >> 2) & 3, p = lane & 3;
    u32 st[16];
#pragma unroll
    for (int k = 0; k < 16; k++) st[k] = 0;
    STAMP(0);
    u64 ep = *(const CAS u64*)(ws + WS_EPOCH);
    u64 tg64 = ep + 0x9E3779B97F4A7C15ull;
    tg64 = (tg64 ^ (tg64 >> 30)) * 0xBF58476D1CE4E5B9ull; tg64 = (tg64 ^ (tg64 >> 27)) * 0x94D049BB133111EBull; tg64 ^= tg64 >> 31;
    const u32 tlo = (u32)tg64, thi = (u32)(tg64 >> 32);

    const rsrc_t rws = __builtin_amdgcn_make_buffer_rsrc(ws, 0, WS_END + 65536 * 4, 0x00020000);
    const u32x4 sx = mksrd(gx, 2048 * 896), sw = mksrd(gwqkv, 448 * 2688), so = mksrd(gwo, 448 * 896), sd = mksrd(gwd, 1792 * 896);
    const bool p3act = s < 28;
    const int tb = 4 * xcd + s / 7, nb = s % 7;
    const u32 vwo = (lane >> 3) * 896 + nb * 128 + 16 * ((lane & 7) ^ (2 * ((lane >> 4) & 3)));
    asm volatile("s_nop 4" ::: "memory");

    if (s < 30) {
        const int idx = (xcd & 1) * 30 + s, blk = idx >> 2, T1 = 4 * (xcd >> 1) + (idx & 3);
        const int mat = blk / 5, j5 = blk % 5;
        const int col0 = 448 * mat + (j5 == 4 ? 352 : 96 * j5);
        const int rot = (blk + 2 * (idx & 3)) % 7;
        const int xr = lane >> 3;
        const u32 vx0 = (128 * T1 + 16 * w + xr) * 896 + 16 * ((lane & 7) ^ (xr >> 1));
        const u32 vx1 = (vx0 ^ 64) + 8 * 896;
        const int sl = (lane >> 2) & 7;
        const u32 vw = (32 * (w >> 2) + 16 * ((w >> 1) & 1) + 8 * (sl >> 2) + 4 * (w & 1) + (sl & 3)) * 2688 + col0 * 2 + 64 * (lane >> 5) + 16 * ((lane & 3) ^ (2 * (sl >> 2)));
        const float qs = mat == 0 ? 0.18898223650461362f * 1.4426950408889634f : 1.f;
        if (mat == 2) p1_body<true>(w, lane, rot, sx, sw, so, vx0, vx1, vw, vwo, p3act, rws, 8 * T1 + w, col0, j5 == 4, qs);
        else p1_body<false>(w, lane, rot, sx, sw, so, vx0, vx1, vw, vwo, p3act, rws, 8 * T1 + w, col0, j5 == 4, qs);
        STAMP(1);
        WAIT_BAR(0);
        if (threadIdx.x == 0) set_flag(rws, WS_FA + (T1 * 16 + blk) * 8, tlo, thi);
    }
    STAMP(2);
    const int tg = 2 * (xcd >> 1) + (s >> 4), fs = 16 * (xcd & 1) + (s & 15);
    const int gl = lane < 56 ? lane : 55;
    const char* gup = ((gl % 14) < 7 ? gwg + (gl % 14) * 16 : gwu + (gl % 14 - 7) * 16) + (4 * w + gl / 14) * 3584 + fs * 112;
    if (p3act) {
#pragma unroll
        for (int k = 0; k < 7; k++) dma(L_WO + (7 * w + k) * 1024, vwo, so, (7 * w + k) * 8 * 896);
    }
#pragma unroll
    for (int a = 8; a < 14; a++) dma_g56(L_GUHI + (32 * (a - 8) + 4 * w) * 224, gup + a * (32 * 3584));

    {
        const int T = 4 * (xcd >> 1) + (s >> 3), h = 8 * (xcd & 1) + (s & 7);
        const int c0 = (7 * h) >> 1;
        {
            int Tp = T - 2 + lane / 15;
            wait_flags(rws, WS_FA + (Tp * 16 + lane % 15) * 8, lane < 45 && Tp >= 0, tlo, thi);
        }
        STAMP(3);
        const bool fg = (h & 1) ? (g == 0) : (g == 3);
        u32x4 qf = ld_sc1(rws, WS_QK + (((8 * T + w) * 112 + c0 + g) * 16 + i) * 16, 0);
        u32x4 kv[6];
#pragma unroll
        for (int k = 0; k < 3; k++) {
            const int n = 3 * w + k;
            const int tt = 8 * T - 16 + n;
            kv[k] = (u32x4){0, 0, 0, 0};
            if (tt >= 0) kv[k] = ld_sc1(rws, WS_QK + ((56 + c0 + g) * 16 + i) * 16, tt * (112 * 256));
            const int G = 4 * T - 8 + (n >> 1);
            int F = 16 * (n & 1) + i; F = F > 27 ? 27 : F;
            kv[3 + k] = (u32x4){0, 0, 0, 0};
            if (G >= 0) kv[3 + k] = ld_sc1(rws, WS_V + (g * 448 + 28 * h + F) * 16, G * (4 * 448 * 16));
        }
#pragma unroll
        for (int k = 0; k < 3; k++) {
            const int n = 3 * w + k;
            const int tt = 8 * T - 16 + n;
            u32x4 kf = kv[k];
            const u32 padv = tt >= 0 ? 0u : 0xF149u;
            if (h & 1) { kf[0] = fg ? padv : kf[0]; kf[1] = fg ? 0u : kf[1]; } else { kf[2] = fg ? padv : kf[2]; kf[3] = fg ? 0u : kf[3]; }
            lds128w(L_K + (2 * (n >> 1) + ((i >> 2) & 1)) * 1024 + (16 * g + 8 * (n & 1) + 4 * (i >> 3) + (i & 3)) * 16, kf);
            u32x4 vf = kv[3 + k];
            if (n & 1) {
                const u32 o1 = i == 12 ? 0x3f803f80u : 0u;
#pragma unroll
                for (int j = 0; j < 4; j++) vf[j] = i >= 12 ? o1 : vf[j];
            }
            lds128w(L_V + n * 1024 + lane * 16, vf);
        }
        if (h & 1) { qf[0] = fg ? 0x3f80u : qf[0]; qf[1] = fg ? 0u : qf[1]; } else { qf[2] = fg ? 0x3f80u : qf[2]; qf[3] = fg ? 0u : qf[3]; }
        BAR();
        STAMP(4);
        f32x4 sc[18];
        const int th = 16 * (w & 1) + i - 8 * g;
        const u32 kb = L_K + (w >> 1) * 2048 + lane * 16;
#pragma unroll
        for (int n = 0; n < 18; n++) {
            f32x4 cin = {0, 0, 0, 0};
            if (n < 2 || n >= 16) {
#pragma unroll
                for (int r = 0; r < 4; r++) { bool le = 4 * (n & 1) + r <= th; cin[r] = (n < 2 ? le : !le) ? -1e30f : 0.f; }
            }
            sc[n] = mfma(lds128(kb + n * 1024), qf, cin);
        }
        float m = -3e38f;
#pragma unroll
        for (int n = 0; n < 18; n++) { m = fmaxf(fmaxf(m, sc[n][0]), sc[n][1]); m = fmaxf(fmaxf(m, sc[n][2]), sc[n][3]); }
        m = xmax(m);
        f32x4 o0 = {0, 0, 0, 0}, o1 = {0, 0, 0, 0};
        const u32 vb = L_V + (w >> 1) * 2048 + lane * 16;
#pragma unroll
        for (int gi = 0; gi < 9; gi++) {
            f32x4 a = sc[2 * gi] - m, c = sc[2 * gi + 1] - m;
            u32x4 pb = {pk2(__builtin_amdgcn_exp2f(a[0]), __builtin_amdgcn_exp2f(a[1])), pk2(__builtin_amdgcn_exp2f(a[2]), __builtin_amdgcn_exp2f(a[3])),
                        pk2(__builtin_amdgcn_exp2f(c[0]), __builtin_amdgcn_exp2f(c[1])), pk2(__builtin_amdgcn_exp2f(c[2]), __builtin_amdgcn_exp2f(c[3]))};
            o0 = mfma(lds128(vb + gi * 2048), pb, o0);
            o1 = mfma(lds128(vb + gi * 2048 + 1024), pb, o1);
        }
        float l = xsum(g == 3 ? o1[0] : 0.f);
        float rl = __builtin_amdgcn_rcpf(l);
        o0 *= rl; o1 *= rl;
        u32x4 of = {pk2(o0[0], o0[1]), pk2(o0[2], o0[3]), g == 3 ? 0u : pk2(o1[0], o1[1]), g == 3 ? 0u : pk2(o1[2], o1[3])};
        st_sc1(rws, of, WS_O + lane * 16, ((8 * T + w) * 16 + h) * 1024);
        STAMP(5);
        WAIT_BAR(0);
        if (threadIdx.x == 0) set_flag(rws, WS_FB + (T * 16 + h) * 8, tlo, thi);
    }
#pragma unroll
    for (int a = 0; a < 8; a++) dma_g56((32 * a + 4 * w) * 224, gup + a * (32 * 3584));
    STAMP(6);

    u32x4 hres = {0, 0, 0, 0};
    const int ttl = w & 3, kh = w >> 2;
    if (p3act) {
        const rsrc_t rx = __builtin_amdgcn_make_buffer_rsrc((void*)gx, 0, 2048 * 896, 0x00020000);
        const u32 xo = (64 * tb + 16 * ttl + i) * 896 + (64 * nb + 32 * kh + 4 * g) * 2;
        u32x2 xr0 = __builtin_amdgcn_raw_buffer_load_b64(rx, xo, 0, 0), xr1 = __builtin_amdgcn_raw_buffer_load_b64(rx, xo + 32, 0, 0);
        u32x4 wf[8][4];
        const int g1 = g == 3 ? 1 : g;
        const u32 fe = 2 * (g & 1) + (q >> 1);
        const u32 r0 = L_WO + (224 * kh + 4 * g + q) * 128 + 8 * p, r1 = L_WO + (224 * kh + 16 + 4 * g1 + q) * 128 + 8 * p;
#pragma unroll
        for (int hh = 0; hh < 8; hh++)
#pragma unroll
            for (int t = 0; t < 4; t++) {
                const u32 sw32 = 32 * ((t ^ (2 * (hh & 1))) ^ fe);
                wf[hh][t] = trfrag(r0 + sw32 + hh * 28 * 128, r1 + sw32 + hh * 28 * 128);
            }
        BAR();
        wait_flags(rws, WS_FB + ((tb >> 1) * 16 + i) * 8, lane < 16, tlo, thi);
        STAMP(7);
        u32x4 of[8];
#pragma unroll
        for (int hh = 0; hh < 8; hh++) of[hh] = ld_sc1(rws, WS_O + lane * 16, ((4 * tb + ttl) * 16 + 8 * kh + hh) * 1024);
        f32x4 acc[4];
#pragma unroll
        for (int t = 0; t < 4; t++) acc[t] = (f32x4){0, 0, 0, 0};
#pragma unroll
        for (int hh = 0; hh < 8; hh++)
#pragma unroll
            for (int t = 0; t < 4; t++) acc[t] = mfma(wf[hh][t], of[hh], acc[t]);
        ldsf4w(L_WO + (w ^ 4) * 2048 + lane * 16, kh ? acc[0] : acc[2]);
        ldsf4w(L_WO + (w ^ 4) * 2048 + 1024 + lane * 16, kh ? acc[1] : acc[3]);
        BAR();
        f32x4 h0 = (kh ? acc[2] : acc[0]) + ldsf4(L_WO + w * 2048 + lane * 16);
        f32x4 h1 = (kh ? acc[3] : acc[1]) + ldsf4(L_WO + w * 2048 + 1024 + lane * 16);
        hres[0] = pk2(h0[0] + bflo(xr0[0]), h0[1] + bfhi(xr0[0])); hres[1] = pk2(h0[2] + bflo(xr0[1]), h0[3] + bfhi(xr0[1]));
        hres[2] = pk2(h1[0] + bflo(xr1[0]), h1[1] + bfhi(xr1[0])); hres[3] = pk2(h1[2] + bflo(xr1[1]), h1[3] + bfhi(xr1[1]));
        st_sc1(rws, hres, WS_H + lane * 16, ((4 * tb + ttl) * 14 + 2 * nb + kh) * 1024);
        STAMP(8);
        WAIT_BAR(0);
        if (threadIdx.x == 0) set_flag(rws, WS_FC + (tb * 8 + nb) * 8, tlo, thi);
    } else {
        WAIT_BAR(0);
    }

    {
        const u32 gb = (4 * g + q) * 224 + 8 * p;
        const u32 mb = (4 * g + q) * 224 + (p < 2 ? 96 : 192) + 8 * p;
        {
            const u32x4 ones = {0x3f803f80u, 0x3f803f80u, 0x3f803f80u, 0x3f803f80u};
            const u32 cb = w >= 6 ? mb : gb + (w < 3 ? 32 * w : 112 + 32 * (w - 3));
            f32x4 cs = {0, 0, 0, 0};
#pragma unroll
            for (int c = 0; c < 14; c++) { const u32 ro = (c < 8 ? 0 : L_GUHI - 57344) + c * 7168; cs = mfma(trfrag(cb + ro, cb + ro + 3584), ones, cs); }
            if (w < 7 && i == 0) ldsf4w(L_SCR + 64 * w + 16 * g, cs);
            BAR();
        }
        wait_flags(rws, WS_FC + ((4 * tg + (w >> 1)) * 8 + i) * 8, lane < 7, tlo, thi);
        STAMP(9);
        const int tt0 = 16 * tg + 2 * w;
        u32x4 hf[2][14];
#pragma unroll
        for (int c = 0; c < 14; c++)
#pragma unroll
            for (int t = 0; t < 2; t++) hf[t][c] = ld_sc1(rws, WS_H + lane * 16, ((tt0 + t) * 14 + c) * 1024);
        f32x4 acc[2][7];
#pragma unroll
        for (int t = 0; t < 2; t++)
#pragma unroll
            for (int n = 0; n < 7; n++) acc[t][n] = (f32x4){0, 0, 0, 0};
        float s1[2] = {0.f, 0.f}, s2[2] = {0.f, 0.f};
#pragma unroll
        for (int c = 0; c < 14; c++) {
            const u32 ro = (c < 8 ? 0 : L_GUHI - 57344) + c * 7168;
#pragma unroll
            for (int n = 0; n < 7; n++) {
                const u32 a = (n == 6 ? mb : gb + (n < 3 ? 32 * n : 112 + 32 * (n - 3))) + ro;
                u32x4 wf = trfrag(a, a + 3584);
                acc[0][n] = mfma(wf, hf[0][c], acc[0][n]);
                acc[1][n] = mfma(wf, hf[1][c], acc[1][n]);
            }
#pragma unroll
            for (int t = 0; t < 2; t++)
#pragma unroll
                for (int j = 0; j < 4; j++) { s1[t] = dot2(hf[t][c][j], 0x3f803f80u, s1[t]); s2[t] = dot2(hf[t][c][j], hf[t][c][j], s2[t]); }
        }
        STAMP(10);
        u32 pa[2][3][2]; f32x4 mm[2];
#pragma unroll
        for (int t = 0; t < 2; t++) {
            float a1 = xsum(s1[t]), a2 = xsum(s2[t]);
            float mean = a1 * (1.f / 448.f);
            float var = fmaxf(a2 * (1.f / 448.f) - mean * mean, 0.f);
            float rstd = __builtin_amdgcn_rsqf(var + 1e-5f), mr = -mean * rstd;
#pragma unroll
            for (int n = 0; n < 3; n++) {
                f32x4 gt = acc[t][n] * rstd + ldsf4(L_SCR + 64 * n + 16 * g) * mr;
                f32x4 up = acc[t][3 + n] * rstd + ldsf4(L_SCR + 64 * (3 + n) + 16 * g) * mr;
                f32x4 av;
#pragma unroll
                for (int r = 0; r < 4; r++) av[r] = gt[r] * up[r] * __builtin_amdgcn_rcpf(1.f + __builtin_amdgcn_exp2f(gt[r] * -1.4426950408889634f));
                pa[t][n][0] = pk2(av[0], av[1]); pa[t][n][1] = pk2(av[2], av[3]);
            }
            mm[t] = acc[t][6] * rstd + ldsf4(L_SCR + 64 * 6 + 16 * g) * mr;
        }
        f32x4 av;
#pragma unroll
        for (int r = 0; r < 4; r++) {
            auto sw2 = __builtin_amdgcn_permlane32_swap(asu(mm[0][r]), asu(mm[1][r]), false, false);
            float gt = asf(sw2[0]), up = asf(sw2[1]);
            av[r] = gt * up * __builtin_amdgcn_rcpf(1.f + __builtin_amdgcn_exp2f(gt * -1.4426950408889634f));
        }
        const u32 m0 = pk2(av[0], av[1]), m1 = pk2(av[2], av[3]);
        const int a4 = fs >> 2, pp = fs & 3;
#pragma unroll
        for (int t = 0; t < 2; t++) {
            u32x4 v = {pa[t][0][0], pa[t][0][1], pa[t][1][0], pa[t][1][1]};
            st_sc1(rws, v, WS_ACT + lane * 16, ((tt0 + t) * 56 + 7 * a4 + pp) * 1024);
        }
        {
            auto x0 = __builtin_amdgcn_permlane32_swap(pa[0][2][0], pa[1][2][0], false, false);
            auto x1 = __builtin_amdgcn_permlane32_swap(pa[0][2][1], pa[1][2][1], false, false);
            u32x4 v = {x0[0], x1[0], x0[1], x1[1]};
            st_sc1(rws, v, WS_ACT + (g >> 1) * (56 * 1024) + ((g & 1) * 16 + i) * 16, (tt0 * 56 + 7 * a4 + 4 + (pp >> 1)) * 1024 + (pp & 1) * 512);
        }
        {
            auto y0 = __builtin_amdgcn_permlane16_swap(m0, m0, false, false);
            auto y1 = __builtin_amdgcn_permlane16_swap(m1, m1, false, false);
            u32x4 v = {y0[0], y1[0], y0[1], y1[1]};
            if ((g & 1) == 0) st_sc1(rws, v, WS_ACT + (g >> 1) * (56 * 1024) + i * 16, (tt0 * 56 + 7 * a4 + 6) * 1024 + pp * 256);
        }
        STAMP(11);
        WAIT_BAR(0);
        if (threadIdx.x == 0) set_flag(rws, WS_FD + (tg * 32 + fs) * 8, tlo, thi);
    }

    if (p3act) {
        u32x4 wd[7][4];
        const u32 wb = 16384 * w;
        const u32 fe = 2 * (g & 1) + (q >> 1);
        const u32 lb = wb + (4 * (g & 1) + q) * 128 + 8 * p;
#pragma unroll
        for (int half = 0; half < 2; half++) {
#pragma unroll
            for (int k = 0; k < 14; k++) {
                const int pl = k / 7, pc = k % 7;
                const u32 dst = pc < 6 ? (pl * 6 + pc) * 1024 : (12 + 2 * half + pl) * 1024;
                dma(wb + dst, vwo, sd, (224 * w + 112 * half + 8 * k) * 896);
            }
            WAIT_VM(0);
#pragma unroll
            for (int t = 0; t < 4; t++) {
                const u32 a = lb + 32 * (t ^ fe);
#pragma unroll
                for (int pl = 0; pl < 2; pl++) wd[2 * half + pl][t] = trfrag(a + (pl * 6 + (g >> 1)) * 1024, a + (pl * 6 + 2 + (g >> 1)) * 1024);
                wd[4 + half][t] = trfrag(a + ((g >> 1) * 6 + 4) * 1024, a + ((g >> 1) * 6 + 5) * 1024);
            }
            asm volatile("s_waitcnt lgkmcnt(0)" ::: "memory");
        }
#pragma unroll
        for (int t = 0; t < 4; t++) {
            const u32 a = wb + (12 + g) * 1024 + q * 128 + 8 * p;
            wd[6][t] = trfrag(a + 32 * (t ^ (q >> 1)), a + 512 + 32 * (t ^ (2 + (q >> 1))));
        }
        BAR();
        STAMP(12);
        wait_flags(rws, WS_FD + ((tb >> 2) * 32 + 4 * w + i) * 8, lane < 4, tlo, thi);
        STAMP(13);
#pragma unroll
        for (int t2 = 0; t2 < 4; t2++) {
            u32x4 af[7];
#pragma unroll
            for (int k = 0; k < 7; k++) af[k] = ld_sc1(rws, WS_ACT + lane * 16, ((4 * tb + t2) * 56 + 7 * w + k) * 1024);
            f32x4 acc[4];
#pragma unroll
            for (int t = 0; t < 4; t++) acc[t] = (f32x4){0, 0, 0, 0};
#pragma unroll
            for (int k = 0; k < 7; k++)
#pragma unroll
                for (int t = 0; t < 4; t++) acc[t] = mfma(wd[k][t], af[k], acc[t]);
#pragma unroll
            for (int t = 0; t < 4; t++) ldsf4w(((t2 * 4 + t) * 8 + w) * 1024 + lane * 16, acc[t]);
        }
        BAR();
        f32x4 r0 = {0, 0, 0, 0}, r1 = {0, 0, 0, 0};
#pragma unroll
        for (int k = 0; k < 8; k++) {
            r0 += ldsf4(((ttl * 4 + 2 * kh) * 8 + k) * 1024 + lane * 16);
            r1 += ldsf4(((ttl * 4 + 2 * kh + 1) * 8 + k) * 1024 + lane * 16);
        }
        u32 a0 = pk2(r0[0] + bflo(hres[0]), r0[1] + bfhi(hres[0])), a1 = pk2(r0[2] + bflo(hres[1]), r0[3] + bfhi(hres[1]));
        u32 b0 = pk2(r1[0] + bflo(hres[2]), r1[1] + bfhi(hres[2])), b1 = pk2(r1[2] + bflo(hres[3]), r1[3] + bfhi(hres[3]));
        auto x0 = __builtin_amdgcn_permlane16_swap(a0, b0, false, false);
        auto x1 = __builtin_amdgcn_permlane16_swap(a1, b1, false, false);
        u32x4 v = {x0[0], x1[0], x0[1], x1[1]};
        *(u32x4*)(gout + (size_t)(64 * tb + 16 * ttl + i) * 896 + (64 * nb + 32 * kh + 8 * (g >> 1) + 16 * (g & 1)) * 2) = v;
        STAMP(14);
    } else if (b == 255 && w == 0) {
#pragma unroll
        for (int k = 0; k < 4; k++) wait_flags(rws, WS_FD + (k * 64 + lane) * 8, true, tlo, thi);
        if (lane == 0) set_flag(rws, WS_EPOCH, tlo, thi);
    }
    asm volatile("s_waitcnt vmcnt(0)" ::: "memory");
    STAMP(15);
    {
        u32 v = 0;
#pragma unroll
        for (int k = 0; k < 16; k++) v = lane == k ? st[k] : v;
        if (lane < 16) *(u32*)(ws + WS_STAMP + ((b * 8 + w) * 16 + lane) * 4) = v;
    }
}

__global__ void report(const char* ws) {
    const u32* st = (const u32*)(ws + WS_STAMP);
    __shared__ u32 sm[3][64];
    int l = threadIdx.x;
    u32 ref = st[0]; int mn = 0x7fffffff;
    for (int i = l; i < 2048; i += 64) { int d = (int)(st[i * 16] - ref); mn = d < mn ? d : mn; }
    for (int o = 32; o; o >>= 1) { int t = __shfl_xor(mn, o); mn = t < mn ? t : mn; }
    u32 t0 = ref + mn;
    int mx = 0, mi = 0x7fffffff; long long sum = 0; int cnt = 0;
    for (int i = l; i < 2048; i += 64) { u32 v = st[i * 16 + SEL]; if (v) { int d = (int)(v - t0); mx = d > mx ? d : mx; mi = d < mi ? d : mi; sum += d; cnt++; } }
    for (int o = 32; o; o >>= 1) { int t = __shfl_xor(mx, o); mx = t > mx ? t : mx; t = __shfl_xor(mi, o); mi = t < mi ? t : mi; sum += __shfl_xor(sum, o); cnt += __shfl_xor(cnt, o); }
    long long val = MODE == 0 ? mx : MODE == 1 ? sum / (cnt ? cnt : 1) : mi;
    if (val > 10000) val = 10000; if (val < 0) val = 0;
    u64 n = (u64)val * AMP;
    u64 s0 = __builtin_amdgcn_s_memrealtime();
    while (__builtin_amdgcn_s_memrealtime() - s0 < n) __builtin_amdgcn_s_sleep(2);
    if (n == 123456789) sm[0][l] = 1;
}
extern "C" void kernel_launch(void* const* d_in, const int* in_sizes, int n_in, void* d_out, int out_size, void* d_ws, size_t ws_size,
                              hipStream_t stream) {
    fused<<<256, 512, 0, stream>>>((const char*)d_in[0], (const char*)d_in[1], (const char*)d_in[2], (const char*)d_in[4],
                                   (const char*)d_in[5], (const char*)d_in[6], (char*)d_out, (char*)d_ws);
    report<<<1, 64, 0, stream>>>((const char*)d_ws);
}
```

```cpp
#define STAMPS
#define SEL 15
#define AMP 0
#define MODE 0
#include <hip/hip_runtime.h>
#include <stdint.h>

typedef unsigned u32;
typedef unsigned long long u64;
typedef u32 u32x2 __attribute__((ext_vector_type(2)));
typedef u32 u32x4 __attribute__((ext_vector_type(4)));
typedef float f32x2 __attribute__((ext_vector_type(2)));
typedef float f32x4 __attribute__((ext_vector_type(4)));
typedef __bf16 bf16x2 __attribute__((ext_vector_type(2)));
typedef __bf16 bf16x8 __attribute__((ext_vector_type(8)));
typedef short s16x4 __attribute__((ext_vector_type(4)));
typedef short s16x8 __attribute__((ext_vector_type(8)));
#define LAS __attribute__((address_space(3)))
#define CAS __attribute__((address_space(4)))
#define DEVI __device__ __forceinline__

constexpr u32 WS_EPOCH = 0;
constexpr u32 WS_FA = 4096;
constexpr u32 WS_FB = WS_FA + 2048;
constexpr u32 WS_FC = WS_FB + 2048;
constexpr u32 WS_FD = WS_FC + 2048;
constexpr u32 WS_QK = 65536;
constexpr u32 WS_V = WS_QK + 128 * 112 * 256;
constexpr u32 WS_O = WS_V + 256 * 448 * 16;
constexpr u32 WS_H = WS_O + 128 * 16 * 1024;
constexpr u32 WS_ACT = WS_H + 128 * 14 * 1024;
constexpr u32 WS_END = WS_ACT + 128 * 56 * 1024;
constexpr u32 WS_STAMP = WS_END;

constexpr u32 L_SLOT = 28672;
constexpr u32 L_WO = 57344;
constexpr u32 L_GUHI = 114688;
constexpr u32 L_SCR = 157696;
constexpr u32 L_K = 0, L_V = 24576;

__shared__ __attribute__((aligned(1024))) char smem[163840];

DEVI f32x4 mfma(u32x4 a, u32x4 b, f32x4 c) {
    return __builtin_amdgcn_mfma_f32_16x16x32_bf16(__builtin_bit_cast(bf16x8, a), __builtin_bit_cast(bf16x8, b), c, 0, 0, 0);
}
DEVI u32x4 trfrag(u32 a0, u32 a1) {
    s16x4 lo = __builtin_amdgcn_ds_read_tr16_b64_v4i16((LAS s16x4*)(smem + a0));
    s16x4 hi = __builtin_amdgcn_ds_read_tr16_b64_v4i16((LAS s16x4*)(smem + a1));
    return __builtin_bit_cast(u32x4, __builtin_shufflevector(lo, hi, 0, 1, 2, 3, 4, 5, 6, 7));
}
DEVI u32x4 lds128(u32 a) { return *(LAS u32x4*)(smem + a); }
DEVI void lds128w(u32 a, u32x4 v) { *(LAS u32x4*)(smem + a) = v; }
DEVI f32x4 ldsf4(u32 a) { return *(LAS f32x4*)(smem + a); }
DEVI void ldsf4w(u32 a, f32x4 v) { *(LAS f32x4*)(smem + a) = v; }
DEVI u32 pk2(float a, float b) { f32x2 f = {a, b}; return __builtin_bit_cast(u32, __builtin_convertvector(f, bf16x2)); }
DEVI float dot2(u32 a, u32 b, float c) { return __builtin_amdgcn_fdot2_f32_bf16(__builtin_bit_cast(bf16x2, a), __builtin_bit_cast(bf16x2, b), c, false); }
DEVI float bflo(u32 v) { return __builtin_bit_cast(float, v << 16); }
DEVI float bfhi(u32 v) { return __builtin_bit_cast(float, v & 0xffff0000u); }
DEVI float asf(u32 v) { return __builtin_bit_cast(float, v); }
DEVI u32 asu(float v) { return __builtin_bit_cast(u32, v); }
DEVI float xsum(float v) {
    u32 u = __builtin_bit_cast(u32, v);
    auto s = __builtin_amdgcn_permlane16_swap(u, u, false, false);
    v = asf(s[0]) + asf(s[1]);
    u = __builtin_bit_cast(u32, v);
    auto t = __builtin_amdgcn_permlane32_swap(u, u, false, false);
    return asf(t[0]) + asf(t[1]);
}
DEVI float xmax(float v) {
    u32 u = __builtin_bit_cast(u32, v);
    auto s = __builtin_amdgcn_permlane16_swap(u, u, false, false);
    v = fmaxf(asf(s[0]), asf(s[1]));
    u = __builtin_bit_cast(u32, v);
    auto t = __builtin_amdgcn_permlane32_swap(u, u, false, false);
    return fmaxf(asf(t[0]), asf(t[1]));
}
DEVI u32x4 mksrd(const void* p, u32 bytes) {
    u64 a = (u64)p;
    u32x4 r = {(u32)a, (u32)(a >> 32) & 0xffffu, bytes, 0x00020000u};
    return r;
}
DEVI void dma(u32 lds, u32 voff, u32x4 srd, u32 soff) {
    u32 keep;
    asm volatile("s_mov_b32 %0, m0\n\ts_mov_b32 m0, %1\n\ts_nop 0\n\tbuffer_load_dwordx4 %2, %3, %4 offen lds\n\ts_mov_b32 m0, %0"
                 : "=&s"(keep) : "s"(lds), "v"(voff), "s"(srd), "s"(soff) : "memory");
}
DEVI void dma_lo32(u32 lds, u32 voff, u32x4 srd, u32 soff) {
    u32 keep;
    asm volatile("s_mov_b32 %0, m0\n\ts_mov_b32 m0, %1\n\ts_mov_b32 exec_hi, 0\n\tbuffer_load_dwordx4 %2, %3, %4 offen lds\n\ts_mov_b32 exec_hi, -1\n\ts_mov_b32 m0, %0"
                 : "=&s"(keep) : "s"(lds), "v"(voff), "s"(srd), "s"(soff) : "memory");
}
DEVI void dma_g56(u32 lds, const char* p) {
    u32 keep;
    asm volatile("s_mov_b32 %0, m0\n\ts_mov_b32 m0, %1\n\ts_mov_b32 exec_hi, 0xffffff\n\tglobal_load_lds_dwordx4 %2, off\n\ts_mov_b32 exec_hi, -1\n\ts_mov_b32 m0, %0"
                 : "=&s"(keep) : "s"(lds), "v"(p) : "memory");
}
#define WAIT_BAR(n) asm volatile("s_waitcnt vmcnt(" #n ") lgkmcnt(0)\n\ts_barrier" ::: "memory")
#define WAIT_VM(n) asm volatile("s_waitcnt vmcnt(" #n ")" ::: "memory")
#define BAR() asm volatile("s_waitcnt lgkmcnt(0)\n\ts_barrier" ::: "memory")

#define STAMP(i) do { st[i] = (u32)__builtin_amdgcn_s_memrealtime(); } while (0)

typedef __amdgpu_buffer_rsrc_t rsrc_t;
DEVI u32x4 ld_sc1(rsrc_t r, u32 voff, u32 soff) { return __builtin_amdgcn_raw_buffer_load_b128(r, voff, soff, 16); }
DEVI void st_sc1(rsrc_t r, u32x4 v, u32 voff, u32 soff) { __builtin_amdgcn_raw_buffer_store_b128(v, r, voff, soff, 16); }

DEVI void wait_flags(rsrc_t r, u32 voff, bool act, u32 tlo, u32 thi) {
    const u64 am = __builtin_amdgcn_ballot_w64(act);
    u32x2 v; u64 sv, ne; u32 cnt;
    asm volatile(
        "s_mov_b64 %[sv], exec\n\ts_mov_b64 exec, %[am]\n\ts_mov_b32 %[cnt], 20000\n"
        "1:\n\tbuffer_load_dwordx2 %[v], %[off], %[r], 0 offen sc1\n\ts_waitcnt vmcnt(0)\n\t"
        "v_cmp_ne_u64 %[ne], %[v], %[tag]\n\ts_cmp_eq_u64 %[ne], 0\n\ts_cbranch_scc1 2f\n\t"
        "s_sleep 1\n\ts_sub_u32 %[cnt], %[cnt], 1\n\ts_cmp_lg_u32 %[cnt], 0\n\ts_cbranch_scc1 1b\n"
        "2:\n\ts_mov_b64 exec, %[sv]"
        : [v] "=&v"(v), [sv] "=&s"(sv), [ne] "=&s"(ne), [cnt] "=&s"(cnt)
        : [off] "v"(voff), [r] "s"(r), [am] "s"(am), [tag] "s"(((u64)thi << 32) | tlo) : "memory", "scc");
}
DEVI void set_flag(rsrc_t r, u32 off, u32 tlo, u32 thi) {
    u32x2 v = {tlo, thi};
    __builtin_amdgcn_raw_buffer_store_b64(v, r, off, 0, 16);
}

template <bool VT>
DEVI void p1_round(u32 sb, u32 xa0, u32 xa1, u32 wa0, u32 wa1, u32 wc, f32x4 (&acc)[6], f32x4& cs, float& s1, float& s2) {
    const u32x4 ones = {0x3f803f80u, 0x3f803f80u, 0x3f803f80u, 0x3f803f80u};
#pragma unroll
    for (int cl = 0; cl < 2; cl++) {
        u32x4 xf = lds128(sb + (cl ? xa1 : xa0));
#pragma unroll
        for (int ct = 0; ct < 6; ct++) {
            u32 o = sb + ((ct & 1) ? wa1 : wa0) + 6144 * cl + 512 * (ct >> 1);
            u32x4 wf = trfrag(o, o + 1536);
            acc[ct] = VT ? mfma(xf, wf, acc[ct]) : mfma(wf, xf, acc[ct]);
        }
        u32 o = sb + wc + 6144 * cl;
        cs = mfma(trfrag(o, o + 1536), ones, cs);
#pragma unroll
        for (int j = 0; j < 4; j++) { s1 = dot2(xf[j], 0x3f803f80u, s1); s2 = dot2(xf[j], xf[j], s2); }
    }
}

template <bool VT>
DEVI void p1_body(int w, int lane, int rot, u32x4 sx, u32x4 sw, u32x4 so, u32 vx0, u32 vx1, u32 vw, u32 vwo, bool p3act,
                  rsrc_t rws, int tt, int col0, bool skip0, float qs) {
    const int i = lane & 15, g = lane >> 4, q = (lane >> 2) & 3, p = lane & 3;
    const u32 xa0 = 2048 * w + 1024 * (i >> 3) + 128 * (i & 7) + 16 * (g ^ ((i >> 1) & 7));
    const u32 xa1 = xa0 ^ 64;
    const u32 wb = 16384 + 3072 * (g >> 1) + 64 * (4 * (g & 1) + q) + 8 * p;
    const u32 wa0 = wb + 32 * (g & 1), wa1 = wb + 32 * ((g & 1) ^ 1);
    const int ctw = w < 6 ? w : w - 6;
    const u32 wc = ((ctw & 1) ? wa1 : wa0) + 512 * (ctw >> 1);
    f32x4 acc[6], cs = {0, 0, 0, 0};
#pragma unroll
    for (int c = 0; c < 6; c++) acc[c] = (f32x4){0, 0, 0, 0};
    float s1 = 0.f, s2 = 0.f;
#define P1_ISSUE(r, slot) do { int kr = (r) + rot; if (kr >= 7) kr -= 7; \
        dma((slot) * L_SLOT + 2048 * w, vx0, sx, kr * 128); dma((slot) * L_SLOT + 2048 * w + 1024, vx1, sx, kr * 128); \
        dma((slot) * L_SLOT + 16384 + 1536 * w, vw, sw, kr * (64 * 2688)); \
        dma_lo32((slot) * L_SLOT + 16384 + 1536 * w + 1024, vw, sw, kr * (64 * 2688) + 128); } while (0)
    P1_ISSUE(0, 0); P1_ISSUE(1, 1); P1_ISSUE(2, 2); P1_ISSUE(3, 3); P1_ISSUE(4, 4);
    WAIT_BAR(16); p1_round<VT>(0 * L_SLOT, xa0, xa1, wa0, wa1, wc, acc, cs, s1, s2);
    WAIT_BAR(12); P1_ISSUE(5, 0); p1_round<VT>(1 * L_SLOT, xa0, xa1, wa0, wa1, wc, acc, cs, s1, s2);
    WAIT_BAR(12); P1_ISSUE(6, 1); p1_round<VT>(2 * L_SLOT, xa0, xa1, wa0, wa1, wc, acc, cs, s1, s2);
    WAIT_BAR(12); p1_round<VT>(3 * L_SLOT, xa0, xa1, wa0, wa1, wc, acc, cs, s1, s2);
    WAIT_BAR(8); p1_round<VT>(4 * L_SLOT, xa0, xa1, wa0, wa1, wc, acc, cs, s1, s2);
    WAIT_BAR(4); p1_round<VT>(0 * L_SLOT, xa0, xa1, wa0, wa1, wc, acc, cs, s1, s2);
    WAIT_BAR(0); p1_round<VT>(1 * L_SLOT, xa0, xa1, wa0, wa1, wc, acc, cs, s1, s2);
#undef P1_ISSUE
    if (w < 6 && i == 0) ldsf4w(L_SCR + 64 * w + 16 * g, cs);
    s1 = xsum(s1); s2 = xsum(s2);
    float mean = s1 * (1.f / 448.f);
    float var = fmaxf(s2 * (1.f / 448.f) - mean * mean, 0.f);
    float rstd = __builtin_amdgcn_rsqf(var + 1e-5f) * qs;
    float mr = -mean * rstd;
    f32x4 rs4, mr4;
    if (VT) {
        if (g == 0) { *(LAS float*)(smem + L_SCR + 512 + 128 * w + 4 * i) = rstd; *(LAS float*)(smem + L_SCR + 512 + 128 * w + 64 + 4 * i) = mr; }
    }
    BAR();
    if (VT) { rs4 = ldsf4(L_SCR + 512 + 128 * w + 16 * g); mr4 = ldsf4(L_SCR + 512 + 128 * w + 64 + 16 * g); }
#pragma unroll
    for (int u = 0; u < 3; u++) {
        u32 pkd[2][2];
#pragma unroll
        for (int t = 0; t < 2; t++) {
            const int ct = 2 * u + t;
            f32x4 v;
            if (VT) { float c = *(LAS float*)(smem + L_SCR + 64 * ct + 4 * i); v = acc[ct] * rs4 + mr4 * c; }
            else { f32x4 c = ldsf4(L_SCR + 64 * ct + 16 * g); v = acc[ct] * rstd + c * mr; }
            pkd[t][0] = pk2(v[0], v[1]); pkd[t][1] = pk2(v[2], v[3]);
        }
        auto a = __builtin_amdgcn_permlane16_swap(pkd[0][0], pkd[1][0], false, false);
        auto b = __builtin_amdgcn_permlane16_swap(pkd[0][1], pkd[1][1], false, false);
        u32x4 o = {a[0], b[0], a[1], b[1]};
        u32 off;
        if (VT) off = WS_V + ((2 * tt + (g >> 1)) * 448 + (col0 - 896) + 32 * u + 16 * (g & 1) + i) * 16;
        else off = WS_QK + ((tt * 112 + (col0 >> 3) + 4 * u + (g >> 1) + 2 * (g & 1)) * 16 + i) * 16;
        if (u > 0 || !skip0) st_sc1(rws, o, off, 0);
    }
}

__global__ void __launch_bounds__(512, 2)
fused(const char* __restrict__ gx, const char* __restrict__ gwqkv, const char* __restrict__ gwo, const char* __restrict__ gwg,
      const char* __restrict__ gwu, const char* __restrict__ gwd, char* gout, char* ws) {
    const int lane = threadIdx.x & 63;
    const int w = __builtin_amdgcn_readfirstlane(threadIdx.x >> 6);
    const int b = blockIdx.x, xcd = b & 7, s = b >> 3;
    const int i = lane & 15, g = lane >> 4, q = (lane >> 2) & 3, p = lane & 3;
    u32 st[16];
#pragma unroll
    for (int k = 0; k < 16; k++) st[k] = 0;
    STAMP(0);
    u64 ep = *(const CAS u64*)(ws + WS_EPOCH);
    u64 tg64 = ep + 0x9E3779B97F4A7C15ull;
    tg64 = (tg64 ^ (tg64 >> 30)) * 0xBF58476D1CE4E5B9ull; tg64 = (tg64 ^ (tg64 >> 27)) * 0x94D049BB133111EBull; tg64 ^= tg64 >> 31;
    const u32 tlo = (u32)tg64, thi = (u32)(tg64 >> 32);

    const rsrc_t rws = __builtin_amdgcn_make_buffer_rsrc(ws, 0, WS_END + 65536 * 4, 0x00020000);
    const u32x4 sx = mksrd(gx, 2048 * 896), sw = mksrd(gwqkv, 448 * 2688), so = mksrd(gwo, 448 * 896), sd = mksrd(gwd, 1792 * 896);
    const bool p3act = s < 28;
    const int tb = 4 * xcd + s / 7, nb = s % 7;
    const u32 vwo = (lane >> 3) * 896 + nb * 128 + 16 * ((lane & 7) ^ (2 * ((lane >> 4) & 3)));
    asm volatile("s_nop 4" ::: "memory");

    if (s < 30) {
        const int idx = (xcd & 1) * 30 + s, blk = idx >> 2, T1 = 4 * (xcd >> 1) + (idx & 3);
        const int mat = blk / 5, j5 = blk % 5;
        const int col0 = 448 * mat + (j5 == 4 ? 352 : 96 * j5);
        const int rot = (blk + 2 * (idx & 3)) % 7;
        const int xr = lane >> 3;
        const u32 vx0 = (128 * T1 + 16 * w + xr) * 896 + 16 * ((lane & 7) ^ (xr >> 1));
        const u32 vx1 = (vx0 ^ 64) + 8 * 896;
        const int sl = (lane >> 2) & 7;
        const u32 vw = (32 * (w >> 2) + 16 * ((w >> 1) & 1) + 8 * (sl >> 2) + 4 * (w & 1) + (sl & 3)) * 2688 + col0 * 2 + 64 * (lane >> 5) + 16 * ((lane & 3) ^ (2 * (sl >> 2)));
        const float qs = mat == 0 ? 0.18898223650461362f * 1.4426950408889634f : 1.f;
        if (mat == 2) p1_body<true>(w, lane, rot, sx, sw, so, vx0, vx1, vw, vwo, p3act, rws, 8 * T1 + w, col0, j5 == 4, qs);
        else p1_body<false>(w, lane, rot, sx, sw, so, vx0, vx1, vw, vwo, p3act, rws, 8 * T1 + w, col0, j5 == 4, qs);
        STAMP(1);
        WAIT_BAR(0);
        if (threadIdx.x == 0) set_flag(rws, WS_FA + (T1 * 16 + blk) * 8, tlo, thi);
    }
    STAMP(2);
    const int tg = 2 * (xcd >> 1) + (s >> 4), fs = 16 * (xcd & 1) + (s & 15);
    const int gl = lane < 56 ? lane : 55;
    const char* gup = ((gl % 14) < 7 ? gwg + (gl % 14) * 16 : gwu + (gl % 14 - 7) * 16) + (4 * w + gl / 14) * 3584 + fs * 112;
    if (p3act) {
#pragma unroll
        for (int k = 0; k < 7; k++) dma(L_WO + (7 * w + k) * 1024, vwo, so, (7 * w + k) * 8 * 896);
    }
#pragma unroll
    for (int a = 8; a < 14; a++) dma_g56(L_GUHI + (32 * (a - 8) + 4 * w) * 224, gup + a * (32 * 3584));

    {
        const int T = 4 * (xcd >> 1) + (s >> 3), h = 8 * (xcd & 1) + (s & 7);
        const int c0 = (7 * h) >> 1;
        {
            int Tp = T - 2 + lane / 15;
            wait_flags(rws, WS_FA + (Tp * 16 + lane % 15) * 8, lane < 45 && Tp >= 0, tlo, thi);
        }
        STAMP(3);
        const bool fg = (h & 1) ? (g == 0) : (g == 3);
        u32x4 qf = ld_sc1(rws, WS_QK + (((8 * T + w) * 112 + c0 + g) * 16 + i) * 16, 0);
        u32x4 kv[6];
#pragma unroll
        for (int k = 0; k < 3; k++) {
            const int n = 3 * w + k;
            const int tt = 8 * T - 16 + n;
            kv[k] = (u32x4){0, 0, 0, 0};
            if (tt >= 0) kv[k] = ld_sc1(rws, WS_QK + ((56 + c0 + g) * 16 + i) * 16, tt * (112 * 256));
            const int G = 4 * T - 8 + (n >> 1);
            int F = 16 * (n & 1) + i; F = F > 27 ? 27 : F;
            kv[3 + k] = (u32x4){0, 0, 0, 0};
            if (G >= 0) kv[3 + k] = ld_sc1(rws, WS_V + (g * 448 + 28 * h + F) * 16, G * (4 * 448 * 16));
        }
#pragma unroll
        for (int k = 0; k < 3; k++) {
            const int n = 3 * w + k;
            const int tt = 8 * T - 16 + n;
            u32x4 kf = kv[k];
            const u32 padv = tt >= 0 ? 0u : 0xF149u;
            if (h & 1) { kf[0] = fg ? padv : kf[0]; kf[1] = fg ? 0u : kf[1]; } else { kf[2] = fg ? padv : kf[2]; kf[3] = fg ? 0u : kf[3]; }
            lds128w(L_K + (2 * (n >> 1) + ((i >> 2) & 1)) * 1024 + (16 * g + 8 * (n & 1) + 4 * (i >> 3) + (i & 3)) * 16, kf);
            u32x4 vf = kv[3 + k];
            if (n & 1) {
                const u32 o1 = i == 12 ? 0x3f803f80u : 0u;
#pragma unroll
                for (int j = 0; j < 4; j++) vf[j] = i >= 12 ? o1 : vf[j];
            }
            lds128w(L_V + n * 1024 + lane * 16, vf);
        }
        if (h & 1) { qf[0] = fg ? 0x3f80u : qf[0]; qf[1] = fg ? 0u : qf[1]; } else { qf[2] = fg ? 0x3f80u : qf[2]; qf[3] = fg ? 0u : qf[3]; }
        BAR();
        STAMP(4);
        f32x4 sc[18];
        const int th = 16 * (w & 1) + i - 8 * g;
        const u32 kb = L_K + (w >> 1) * 2048 + lane * 16;
#pragma unroll
        for (int n = 0; n < 18; n++) {
            f32x4 cin = {0, 0, 0, 0};
            if (n < 2 || n >= 16) {
#pragma unroll
                for (int r = 0; r < 4; r++) { bool le = 4 * (n & 1) + r <= th; cin[r] = (n < 2 ? le : !le) ? -1e30f : 0.f; }
            }
            sc[n] = mfma(lds128(kb + n * 1024), qf, cin);
        }
        float m = -3e38f;
#pragma unroll
        for (int n = 0; n < 18; n++) { m = fmaxf(fmaxf(m, sc[n][0]), sc[n][1]); m = fmaxf(fmaxf(m, sc[n][2]), sc[n][3]); }
        m = xmax(m);
        f32x4 o0 = {0, 0, 0, 0}, o1 = {0, 0, 0, 0};
        const u32 vb = L_V + (w >> 1) * 2048 + lane * 16;
#pragma unroll
        for (int gi = 0; gi < 9; gi++) {
            f32x4 a = sc[2 * gi] - m, c = sc[2 * gi + 1] - m;
            u32x4 pb = {pk2(__builtin_amdgcn_exp2f(a[0]), __builtin_amdgcn_exp2f(a[1])), pk2(__builtin_amdgcn_exp2f(a[2]), __builtin_amdgcn_exp2f(a[3])),
                        pk2(__builtin_amdgcn_exp2f(c[0]), __builtin_amdgcn_exp2f(c[1])), pk2(__builtin_amdgcn_exp2f(c[2]), __builtin_amdgcn_exp2f(c[3]))};
            o0 = mfma(lds128(vb + gi * 2048), pb, o0);
            o1 = mfma(lds128(vb + gi * 2048 + 1024), pb, o1);
        }
        float l = xsum(g == 3 ? o1[0] : 0.f);
        float rl = __builtin_amdgcn_rcpf(l);
        o0 *= rl; o1 *= rl;
        u32x4 of = {pk2(o0[0], o0[1]), pk2(o0[2], o0[3]), g == 3 ? 0u : pk2(o1[0], o1[1]), g == 3 ? 0u : pk2(o1[2], o1[3])};
        st_sc1(rws, of, WS_O + lane * 16, ((8 * T + w) * 16 + h) * 1024);
        STAMP(5);
        WAIT_BAR(0);
        if (threadIdx.x == 0) set_flag(rws, WS_FB + (T * 16 + h) * 8, tlo, thi);
    }
#pragma unroll
    for (int a = 0; a < 8; a++) dma_g56((32 * a + 4 * w) * 224, gup + a * (32 * 3584));
    STAMP(6);

    u32x4 hres = {0, 0, 0, 0};
    const int ttl = w & 3, kh = w >> 2;
    if (p3act) {
        const rsrc_t rx = __builtin_amdgcn_make_buffer_rsrc((void*)gx, 0, 2048 * 896, 0x00020000);
        const u32 xo = (64 * tb + 16 * ttl + i) * 896 + (64 * nb + 32 * kh + 4 * g) * 2;
        u32x2 xr0 = __builtin_amdgcn_raw_buffer_load_b64(rx, xo, 0, 0), xr1 = __builtin_amdgcn_raw_buffer_load_b64(rx, xo + 32, 0, 0);
        u32x4 wf[8][4];
        const int g1 = g == 3 ? 1 : g;
        const u32 fe = 2 * (g & 1) + (q >> 1);
        const u32 r0 = L_WO + (224 * kh + 4 * g + q) * 128 + 8 * p, r1 = L_WO + (224 * kh + 16 + 4 * g1 + q) * 128 + 8 * p;
#pragma unroll
        for (int hh = 0; hh < 8; hh++)
#pragma unroll
            for (int t = 0; t < 4; t++) {
                const u32 sw32 = 32 * ((t ^ (2 * (hh & 1))) ^ fe);
                wf[hh][t] = trfrag(r0 + sw32 + hh * 28 * 128, r1 + sw32 + hh * 28 * 128);
            }
        BAR();
        wait_flags(rws, WS_FB + ((tb >> 1) * 16 + i) * 8, lane < 16, tlo, thi);
        STAMP(7);
        u32x4 of[8];
#pragma unroll
        for (int hh = 0; hh < 8; hh++) of[hh] = ld_sc1(rws, WS_O + lane * 16, ((4 * tb + ttl) * 16 + 8 * kh + hh) * 1024);
        f32x4 acc[4];
#pragma unroll
        for (int t = 0; t < 4; t++) acc[t] = (f32x4){0, 0, 0, 0};
#pragma unroll
        for (int hh = 0; hh < 8; hh++)
#pragma unroll
            for (int t = 0; t < 4; t++) acc[t] = mfma(wf[hh][t], of[hh], acc[t]);
        ldsf4w(L_WO + (w ^ 4) * 2048 + lane * 16, kh ? acc[0] : acc[2]);
        ldsf4w(L_WO + (w ^ 4) * 2048 + 1024 + lane * 16, kh ? acc[1] : acc[3]);
        BAR();
        f32x4 h0 = (kh ? acc[2] : acc[0]) + ldsf4(L_WO + w * 2048 + lane * 16);
        f32x4 h1 = (kh ? acc[3] : acc[1]) + ldsf4(L_WO + w * 2048 + 1024 + lane * 16);
        hres[0] = pk2(h0[0] + bflo(xr0[0]), h0[1] + bfhi(xr0[0])); hres[1] = pk2(h0[2] + bflo(xr0[1]), h0[3] + bfhi(xr0[1]));
        hres[2] = pk2(h1[0] + bflo(xr1[0]), h1[1] + bfhi(xr1[0])); hres[3] = pk2(h1[2] + bflo(xr1[1]), h1[3] + bfhi(xr1[1]));
        st_sc1(rws, hres, WS_H + lane * 16, ((4 * tb + ttl) * 14 + 2 * nb + kh) * 1024);
        STAMP(8);
        WAIT_BAR(0);
        if (threadIdx.x == 0) set_flag(rws, WS_FC + (tb * 8 + nb) * 8, tlo, thi);
    } else {
        WAIT_BAR(0);
    }

    {
        const u32 gb = (4 * g + q) * 224 + 8 * p;
        const u32 mb = (4 * g + q) * 224 + (p < 2 ? 96 : 192) + 8 * p;
        {
            const u32x4 ones = {0x3f803f80u, 0x3f803f80u, 0x3f803f80u, 0x3f803f80u};
            const u32 cb = w >= 6 ? mb : gb + (w < 3 ? 32 * w : 112 + 32 * (w - 3));
            f32x4 cs = {0, 0, 0, 0};
#pragma unroll
            for (int c = 0; c < 14; c++) { const u32 ro = (c < 8 ? 0 : L_GUHI - 57344) + c * 7168; cs = mfma(trfrag(cb + ro, cb + ro + 3584), ones, cs); }
            if (w < 7 && i == 0) ldsf4w(L_SCR + 64 * w + 16 * g, cs);
            BAR();
        }
        wait_flags(rws, WS_FC + ((4 * tg + (w >> 1)) * 8 + i) * 8, lane < 7, tlo, thi);
        STAMP(9);
        const int tt0 = 16 * tg + 2 * w;
        u32x4 hf[2][14];
#pragma unroll
        for (int c = 0; c < 14; c++)
#pragma unroll
            for (int t = 0; t < 2; t++) hf[t][c] = ld_sc1(rws, WS_H + lane * 16, ((tt0 + t) * 14 + c) * 1024);
        f32x4 acc[2][7];
#pragma unroll
        for (int t = 0; t < 2; t++)
#pragma unroll
            for (int n = 0; n < 7; n++) acc[t][n] = (f32x4){0, 0, 0, 0};
        float s1[2] = {0.f, 0.f}, s2[2] = {0.f, 0.f};
#pragma unroll
        for (int c = 0; c < 14; c++) {
            const u32 ro = (c < 8 ? 0 : L_GUHI - 57344) + c * 7168;
#pragma unroll
            for (int n = 0; n < 7; n++) {
                const u32 a = (n == 6 ? mb : gb + (n < 3 ? 32 * n : 112 + 32 * (n - 3))) + ro;
                u32x4 wf = trfrag(a, a + 3584);
                acc[0][n] = mfma(wf, hf[0][c], acc[0][n]);
                acc[1][n] = mfma(wf, hf[1][c], acc[1][n]);
            }
#pragma unroll
            for (int t = 0; t < 2; t++)
#pragma unroll
                for (int j = 0; j < 4; j++) { s1[t] = dot2(hf[t][c][j], 0x3f803f80u, s1[t]); s2[t] = dot2(hf[t][c][j], hf[t][c][j], s2[t]); }
        }
        STAMP(10);
        u32 pa[2][3][2]; f32x4 mm[2];
#pragma unroll
        for (int t = 0; t < 2; t++) {
            float a1 = xsum(s1[t]), a2 = xsum(s2[t]);
            float mean = a1 * (1.f / 448.f);
            float var = fmaxf(a2 * (1.f / 448.f) - mean * mean, 0.f);
            float rstd = __builtin_amdgcn_rsqf(var + 1e-5f), mr = -mean * rstd;
#pragma unroll
            for (int n = 0; n < 3; n++) {
                f32x4 gt = acc[t][n] * rstd + ldsf4(L_SCR + 64 * n + 16 * g) * mr;
                f32x4 up = acc[t][3 + n] * rstd + ldsf4(L_SCR + 64 * (3 + n) + 16 * g) * mr;
                f32x4 av;
#pragma unroll
                for (int r = 0; r < 4; r++) av[r] = gt[r] * up[r] * __builtin_amdgcn_rcpf(1.f + __builtin_amdgcn_exp2f(gt[r] * -1.4426950408889634f));
                pa[t][n][0] = pk2(av[0], av[1]); pa[t][n][1] = pk2(av[2], av[3]);
            }
            mm[t] = acc[t][6] * rstd + ldsf4(L_SCR + 64 * 6 + 16 * g) * mr;
        }
        f32x4 av;
#pragma unroll
        for (int r = 0; r < 4; r++) {
            auto sw2 = __builtin_amdgcn_permlane32_swap(asu(mm[0][r]), asu(mm[1][r]), false, false);
            float gt = asf(sw2[0]), up = asf(sw2[1]);
            av[r] = gt * up * __builtin_amdgcn_rcpf(1.f + __builtin_amdgcn_exp2f(gt * -1.4426950408889634f));
        }
        const u32 m0 = pk2(av[0], av[1]), m1 = pk2(av[2], av[3]);
        const int a4 = fs >> 2, pp = fs & 3;
#pragma unroll
        for (int t = 0; t < 2; t++) {
            u32x4 v = {pa[t][0][0], pa[t][0][1], pa[t][1][0], pa[t][1][1]};
            st_sc1(rws, v, WS_ACT + lane * 16, ((tt0 + t) * 56 + 7 * a4 + pp) * 1024);
        }
        {
            auto x0 = __builtin_amdgcn_permlane32_swap(pa[0][2][0], pa[1][2][0], false, false);
            auto x1 = __builtin_amdgcn_permlane32_swap(pa[0][2][1], pa[1][2][1], false, false);
            u32x4 v = {x0[0], x1[0], x0[1], x1[1]};
            st_sc1(rws, v, WS_ACT + (g >> 1) * (56 * 1024) + ((g & 1) * 16 + i) * 16, (tt0 * 56 + 7 * a4 + 4 + (pp >> 1)) * 1024 + (pp & 1) * 512);
        }
        {
            auto y0 = __builtin_amdgcn_permlane16_swap(m0, m0, false, false);
            auto y1 = __builtin_amdgcn_permlane16_swap(m1, m1, false, false);
            u32x4 v = {y0[0], y1[0], y0[1], y1[1]};
            if ((g & 1) == 0) st_sc1(rws, v, WS_ACT + (g >> 1) * (56 * 1024) + i * 16, (tt0 * 56 + 7 * a4 + 6) * 1024 + pp * 256);
        }
        STAMP(11);
        WAIT_BAR(0);
        if (threadIdx.x == 0) set_flag(rws, WS_FD + (tg * 32 + fs) * 8, tlo, thi);
    }

    if (p3act) {
        u32x4 wd[7][4];
        const u32 wb = 16384 * w;
        const u32 fe = 2 * (g & 1) + (q >> 1);
        const u32 lb = wb + (4 * (g & 1) + q) * 128 + 8 * p;
#pragma unroll
        for (int half = 0; half < 2; half++) {
#pragma unroll
            for (int k = 0; k < 14; k++) {
                const int pl = k / 7, pc = k % 7;
                const u32 dst = pc < 6 ? (pl * 6 + pc) * 1024 : (12 + 2 * half + pl) * 1024;
                dma(wb + dst, vwo, sd, (224 * w + 112 * half + 8 * k) * 896);
            }
            WAIT_VM(0);
#pragma unroll
            for (int t = 0; t < 4; t++) {
                const u32 a = lb + 32 * (t ^ fe);
#pragma unroll
                for (int pl = 0; pl < 2; pl++) wd[2 * half + pl][t] = trfrag(a + (pl * 6 + (g >> 1)) * 1024, a + (pl * 6 + 2 + (g >> 1)) * 1024);
                wd[4 + half][t] = trfrag(a + ((g >> 1) * 6 + 4) * 1024, a + ((g >> 1) * 6 + 5) * 1024);
            }
            asm volatile("s_waitcnt lgkmcnt(0)" ::: "memory");
        }
#pragma unroll
        for (int t = 0; t < 4; t++) {
            const u32 a = wb + (12 + g) * 1024 + q * 128 + 8 * p;
            wd[6][t] = trfrag(a + 32 * (t ^ (q >> 1)), a + 512 + 32 * (t ^ (2 + (q >> 1))));
        }
        BAR();
        STAMP(12);
        wait_flags(rws, WS_FD + ((tb >> 2) * 32 + 4 * w + i) * 8, lane < 4, tlo, thi);
        STAMP(13);
#pragma unroll
        for (int t2 = 0; t2 < 4; t2++) {
            u32x4 af[7];
#pragma unroll
            for (int k = 0; k < 7; k++) af[k] = ld_sc1(rws, WS_ACT + lane * 16, ((4 * tb + t2) * 56 + 7 * w + k) * 1024);
            f32x4 acc[4];
#pragma unroll
            for (int t = 0; t < 4; t++) acc[t] = (f32x4){0, 0, 0, 0};
#pragma unroll
            for (int k = 0; k < 7; k++)
#pragma unroll
                for (int t = 0; t < 4; t++) acc[t] = mfma(wd[k][t], af[k], acc[t]);
#pragma unroll
            for (int t = 0; t < 4; t++) ldsf4w(((t2 * 4 + t) * 8 + w) * 1024 + lane * 16, acc[t]);
        }
        BAR();
        f32x4 r0 = {0, 0, 0, 0}, r1 = {0, 0, 0, 0};
#pragma unroll
        for (int k = 0; k < 8; k++) {
            r0 += ldsf4(((ttl * 4 + 2 * kh) * 8 + k) * 1024 + lane * 16);
            r1 += ldsf4(((ttl * 4 + 2 * kh + 1) * 8 + k) * 1024 + lane * 16);
        }
        u32 a0 = pk2(r0[0] + bflo(hres[0]), r0[1] + bfhi(hres[0])), a1 = pk2(r0[2] + bflo(hres[1]), r0[3] + bfhi(hres[1]));
        u32 b0 = pk2(r1[0] + bflo(hres[2]), r1[1] + bfhi(hres[2])), b1 = pk2(r1[2] + bflo(hres[3]), r1[3] + bfhi(hres[3]));
        auto x0 = __builtin_amdgcn_permlane16_swap(a0, b0, false, false);
        auto x1 = __builtin_amdgcn_permlane16_swap(a1, b1, false, false);
        u32x4 v = {x0[0], x1[0], x0[1], x1[1]};
        *(u32x4*)(gout + (size_t)(64 * tb + 16 * ttl + i) * 896 + (64 * nb + 32 * kh + 8 * (g >> 1) + 16 * (g & 1)) * 2) = v;
        STAMP(14);
    } else if (b == 255 && w == 0) {
#pragma unroll
        for (int k = 0; k < 4; k++) wait_flags(rws, WS_FD + (k * 64 + lane) * 8, true, tlo, thi);
        if (lane == 0) set_flag(rws, WS_EPOCH, tlo, thi);
    }
    asm volatile("s_waitcnt vmcnt(0)" ::: "memory");
    STAMP(15);
    {
        u32 v = 0;
#pragma unroll
        for (int k = 0; k < 16; k++) v = lane == k ? st[k] : v;
        if (lane < 16) *(u32*)(ws + WS_STAMP + ((b * 8 + w) * 16 + lane) * 4) = v;
    }
}

__global__ void prek(char* ws) { if (threadIdx.x == 0) *(u32*)(ws + WS_STAMP + 200000) = (u32)__builtin_amdgcn_s_memrealtime(); }
__global__ void report(const char* ws) {
    u32 rs = (u32)__builtin_amdgcn_s_memrealtime();
    const u32* st = (const u32*)(ws + WS_STAMP);
    int l = threadIdx.x;
    u32 ref = st[0]; int mn = 0x7fffffff;
    for (int i = l; i < 2048; i += 64) { int d = (int)(st[i * 16] - ref); mn = d < mn ? d : mn; }
    for (int o = 32; o; o >>= 1) { int t = __shfl_xor(mn, o); mn = t < mn ? t : mn; }
    u32 t0 = ref + mn;
    int mx = 0, mi = 0x7fffffff; long long sum = 0; int cnt = 0;
    for (int i = l; i < 2048; i += 64) { u32 v = st[i * 16 + SEL]; if (v) { int d = (int)(v - t0); mx = d > mx ? d : mx; mi = d < mi ? d : mi; sum += d; cnt++; } }
    for (int o = 32; o; o >>= 1) { int t = __shfl_xor(mx, o); mx = t > mx ? t : mx; t = __shfl_xor(mi, o); mi = t < mi ? t : mi; sum += __shfl_xor(sum, o); cnt += __shfl_xor(cnt, o); }
    long long val = MODE == 0 ? mx : MODE == 1 ? sum / (cnt ? cnt : 1) : MODE == 2 ? mi : MODE == 3 ? (int)(t0 - st[50000]) : (int)(rs - t0) - mx;
    if (val > 10000) val = 10000; if (val < 0) val = 0;
    int amp = AMP; asm volatile("" : "+s"(amp));
    u32 n = (u32)val * amp + 1500;
    while ((u32)__builtin_amdgcn_s_memrealtime() - rs < n) __builtin_amdgcn_s_sleep(2);
}
extern "C" void kernel_launch(void* const* d_in, const int* in_sizes, int n_in, void* d_out, int out_size, void* d_ws, size_t ws_size,
                              hipStream_t stream) {
    fused<<<256, 512, 0, stream>>>((const char*)d_in[0], (const char*)d_in[1], (const char*)d_in[2], (const char*)d_in[4],
                                   (const char*)d_in[5], (const char*)d_in[6], (char*)d_out, (char*)d_ws);
    report<<<1, 64, 0, stream>>>((const char*)d_ws);
}
```

```cpp
#include <hip/hip_runtime.h>
#include <stdint.h>

typedef unsigned u32;
typedef unsigned long long u64;
typedef u32 u32x2 __attribute__((ext_vector_type(2)));
typedef u32 u32x4 __attribute__((ext_vector_type(4)));
typedef float f32x2 __attribute__((ext_vector_type(2)));
typedef float f32x4 __attribute__((ext_vector_type(4)));
typedef __bf16 bf16x2 __attribute__((ext_vector_type(2)));
typedef __bf16 bf16x8 __attribute__((ext_vector_type(8)));
typedef short s16x4 __attribute__((ext_vector_type(4)));
typedef short s16x8 __attribute__((ext_vector_type(8)));
#define LAS __attribute__((address_space(3)))
#define CAS __attribute__((address_space(4)))
#define DEVI __device__ __forceinline__

constexpr u32 WS_EPOCH = 0;
constexpr u32 WS_FA = 4096;
constexpr u32 WS_FC = 32768;
constexpr u32 WS_FW = 90112;
constexpr u32 WS_FB = 131072;
constexpr u32 WS_FD = 393216;
constexpr u32 WS_ST = 655360;
constexpr u32 WS_QKV = 1048576;
constexpr u32 WS_O = WS_QKV + 128 * 168 * 256;
constexpr u32 WS_H = WS_O + 128 * 16 * 1024;
constexpr u32 WS_ACT = WS_H + 128 * 14 * 1024;
constexpr u32 WS_WT = WS_ACT + 128 * 56 * 1024;
constexpr u32 WS_END = WS_WT + 7 * 8 * 28 * 1024;
constexpr u32 WS_STAMP = WS_END;

constexpr u32 L_WO = 57344;
constexpr u32 L_GUHI = 114688;
constexpr u32 L_SCR = 157696;
constexpr u32 L_K = 0, L_V = 24576, L_Q = 49152;

__shared__ __attribute__((aligned(1024))) char smem[163840];

DEVI f32x4 mfma(u32x4 a, u32x4 b, f32x4 c) {
    return __builtin_amdgcn_mfma_f32_16x16x32_bf16(__builtin_bit_cast(bf16x8, a), __builtin_bit_cast(bf16x8, b), c, 0, 0, 0);
}
DEVI u32x2 trhalf(u32 a0) { return __builtin_bit_cast(u32x2, __builtin_amdgcn_ds_read_tr16_b64_v4i16((LAS s16x4*)(smem + a0))); }
DEVI u32x4 trfrag(u32 a0, u32 a1) {
    s16x4 lo = __builtin_amdgcn_ds_read_tr16_b64_v4i16((LAS s16x4*)(smem + a0));
    s16x4 hi = __builtin_amdgcn_ds_read_tr16_b64_v4i16((LAS s16x4*)(smem + a1));
    return __builtin_bit_cast(u32x4, __builtin_shufflevector(lo, hi, 0, 1, 2, 3, 4, 5, 6, 7));
}
DEVI u32x4 lds128(u32 a) { return *(LAS u32x4*)(smem + a); }
DEVI void lds128w(u32 a, u32x4 v) { *(LAS u32x4*)(smem + a) = v; }
DEVI f32x4 ldsf4(u32 a) { return *(LAS f32x4*)(smem + a); }
DEVI void ldsf4w(u32 a, f32x4 v) { *(LAS f32x4*)(smem + a) = v; }
DEVI f32x2 ldsf2(u32 a) { return *(LAS f32x2*)(smem + a); }
DEVI void ldsf2w(u32 a, f32x2 v) { *(LAS f32x2*)(smem + a) = v; }
DEVI u32 pk2(float a, float b) { f32x2 f = {a, b}; return __builtin_bit_cast(u32, __builtin_convertvector(f, bf16x2)); }
DEVI float dot2(u32 a, u32 b, float c) { return __builtin_amdgcn_fdot2_f32_bf16(__builtin_bit_cast(bf16x2, a), __builtin_bit_cast(bf16x2, b), c, false); }
DEVI float bflo(u32 v) { return __builtin_bit_cast(float, v << 16); }
DEVI float bfhi(u32 v) { return __builtin_bit_cast(float, v & 0xffff0000u); }
DEVI float asf(u32 v) { return __builtin_bit_cast(float, v); }
DEVI u32 asu(float v) { return __builtin_bit_cast(u32, v); }
DEVI float xsum(float v) {
    u32 u = asu(v);
    auto s = __builtin_amdgcn_permlane16_swap(u, u, false, false);
    v = asf(s[0]) + asf(s[1]);
    u = asu(v);
    auto t = __builtin_amdgcn_permlane32_swap(u, u, false, false);
    return asf(t[0]) + asf(t[1]);
}
DEVI float xmax(float v) {
    u32 u = asu(v);
    auto s = __builtin_amdgcn_permlane16_swap(u, u, false, false);
    v = fmaxf(asf(s[0]), asf(s[1]));
    u = asu(v);
    auto t = __builtin_amdgcn_permlane32_swap(u, u, false, false);
    return fmaxf(asf(t[0]), asf(t[1]));
}
DEVI u32x4 mksrd(const void* p, u32 bytes) {
    u64 a = (u64)p;
    u32x4 r = {(u32)a, (u32)(a >> 32) & 0xffffu, bytes, 0x00020000u};
    return r;
}
DEVI void dma(u32 lds, u32 voff, u32x4 srd, u32 soff) {
    asm volatile("s_mov_b32 m0, %0\n\ts_nop 0\n\tbuffer_load_dwordx4 %1, %2, %3 offen lds" :: "s"(lds), "v"(voff), "s"(srd), "s"(soff) : "memory");
}
DEVI void dma_sc1(u32 lds, u32 voff, u32x4 srd, u32 soff) {
    asm volatile("s_mov_b32 m0, %0\n\ts_nop 0\n\tbuffer_load_dwordx4 %1, %2, %3 offen sc1 lds" :: "s"(lds), "v"(voff), "s"(srd), "s"(soff) : "memory");
}
DEVI void dma_lo32(u32 lds, u32 voff, u32x4 srd, u32 soff) {
    asm volatile("s_mov_b32 m0, %0\n\ts_mov_b32 exec_hi, 0\n\tbuffer_load_dwordx4 %1, %2, %3 offen lds\n\ts_mov_b32 exec_hi, -1"
                 :: "s"(lds), "v"(voff), "s"(srd), "s"(soff) : "memory");
}
DEVI void dma_g56(u32 lds, const char* p) {
    asm volatile("s_mov_b32 m0, %0\n\ts_mov_b32 exec_hi, 0xffffff\n\tglobal_load_lds_dwordx4 %1, off\n\ts_mov_b32 exec_hi, -1" :: "s"(lds), "v"(p) : "memory");
}
#define WAIT_BAR(n) asm volatile("s_waitcnt vmcnt(%0) lgkmcnt(0)\n\ts_barrier" :: "n"(n) : "memory")
#define WAIT_VM(n) asm volatile("s_waitcnt vmcnt(%0)" :: "n"(n) : "memory")
#define BAR() asm volatile("s_waitcnt lgkmcnt(0)\n\ts_barrier" ::: "memory")
#define SB() __builtin_amdgcn_sched_barrier(0)

#define STAMP(i) do {} while (0)
#define STAMPG(grp, i) do {} while (0)

typedef __amdgpu_buffer_rsrc_t rsrc_t;
DEVI u32x4 ld_sc1(rsrc_t r, u32 voff, u32 soff) { return __builtin_amdgcn_raw_buffer_load_b128(r, voff, soff, 16); }
DEVI u32x2 ld2_sc1(rsrc_t r, u32 voff, u32 soff) { return __builtin_amdgcn_raw_buffer_load_b64(r, voff, soff, 16); }
DEVI void st_sc1(rsrc_t r, u32x4 v, u32 voff, u32 soff) { __builtin_amdgcn_raw_buffer_store_b128(v, r, voff + soff, 0, 16); }

DEVI void wait_flags(rsrc_t r, u32 voff, u64 am, u64 tag) {
    u32x2 v; u64 ne; u32 cnt;
    asm volatile(
        "s_mov_b64 exec, %[am]\n\ts_mov_b32 %[cnt], 20000\n"
        "1:\n\tbuffer_load_dwordx2 %[v], %[off], %[r], 0 offen sc1\n\ts_waitcnt vmcnt(0)\n\t"
        "v_cmp_ne_u64 %[ne], %[v], %[tag]\n\ts_cmp_eq_u64 %[ne], 0\n\ts_cbranch_scc1 2f\n\t"
        "s_sub_u32 %[cnt], %[cnt], 1\n\ts_cmp_lg_u32 %[cnt], 0\n\ts_cbranch_scc1 1b\n"
        "2:\n\ts_mov_b64 exec, -1"
        : [v] "=&v"(v), [ne] "=&s"(ne), [cnt] "=&s"(cnt)
        : [off] "v"(voff), [r] "s"(r), [am] "s"(am), [tag] "s"(tag) : "memory", "scc");
}
DEVI void set_flags(rsrc_t r, u32 voff, u64 am, u64 tag) {
    asm volatile("s_mov_b64 exec, %[am]\n\tbuffer_store_dwordx2 %[v], %[off], %[r], 0 offen sc1\n\ts_mov_b64 exec, -1"
                 :: [v] "v"(tag), [off] "v"(voff), [r] "s"(r), [am] "s"(am) : "memory");
}

#define LA 3
#define NWT1 14
#define NWT2 0
#define HW 4
#define PD 3

#ifdef __HIP_DEVICE_COMPILE__
#define NOPK __attribute__((target("no-packed-fp32-ops")))
#else
#define NOPK
#endif
__global__ void __launch_bounds__(512, 2) NOPK
fused(const char* __restrict__ gx, const char* __restrict__ gwqkv, const char* __restrict__ gwo, const char* __restrict__ gwg,
      const char* __restrict__ gwu, const char* __restrict__ gwd, char* gout, char* ws) {
    const int lane = threadIdx.x & 63;
    const int w = __builtin_amdgcn_readfirstlane(threadIdx.x >> 6);
    const int b = blockIdx.x, xcd = b & 7, s = b >> 3;
    const int i = lane & 15, g = lane >> 4, q = (lane >> 2) & 3, p = lane & 3;
    const u32x4 ones = {0x3f803f80u, 0x3f803f80u, 0x3f803f80u, 0x3f803f80u};
    STAMP(0);
    u32 cpf;
    {
        u64 pc; u32 len;
        asm volatile("s_getpc_b64 %0\n.Lfz_a:\n\ts_mov_b32 %1, .Lfz_end-.Lfz_a-4" : "=s"(pc), "=s"(len));
        u32 o = threadIdx.x * 128; o = o < len ? o : len;
        asm volatile("global_load_dword %0, %1, %2" : "=v"(cpf) : "v"(o), "s"(pc) : "memory");
    }
    const rsrc_t rws = __builtin_amdgcn_make_buffer_rsrc(ws, 0, WS_END + 65536 * 4, 0x00020000);
    const u32x4 sws = mksrd(ws, WS_END);
    const u32x4 so = mksrd(gwo, 448 * 896);
    const bool p3act = s < 28;
    const int tb = 4 * xcd + s / 7, nb = s % 7;
    const int T = 4 * (xcd >> 1) + (s >> 3), h = 8 * (xcd & 1) + (s & 7);
    const int tg = 4 * (xcd >> 2) + (s >> 3), fs = 8 * (xcd & 3) + (s & 7);
    const int wl = w & 3, hf = w >> 2;
    const int blk = 7 * (xcd & 1) + (s >> 2), T1 = 4 * (xcd >> 1) + (s & 3);
    const int fsh = (s - 28) * 8 + xcd;
    u32 pwo;
    {
        u32 a = lane < 8 ? WS_FB + ((8 * T + w) * 16 + h) * 128 : WS_FD + (((4 * tg + wl) * 2 + hf) * 32 + fs) * 128;
        u32 c = p3act ? (w == 0 ? WS_FA + (T1 * 14 + blk) * 128 : WS_FC + ((tb * 7 + nb) * 2 + (w & 1)) * 128) : WS_FW + fsh * 128;
        pwo = (lane < 16 ? a : c) + (lane & 7) * 16;
    }
    const u64 pwm = w < 3 ? 0xffffffull : 0xffffull;
#define PREWARM() asm volatile("s_mov_b64 exec, %2\n\tbuffer_store_dwordx4 %0, %1, %3, 0 offen sc1\n\ts_mov_b64 exec, -1" :: "v"(ones), "v"(pwo), "s"(pwm), "s"(rws) : "memory")
    const u32 vwo = (lane >> 3) * 896 + nb * 128 + 16 * ((lane & 7) ^ (2 * ((lane >> 4) & 3)));
    u64 tag;

    if (p3act) {
        const u32x4 sx = mksrd(gx, 2048 * 896), sw = mksrd(gwqkv, 448 * 2688);
        const int rot = ((s >> 2) + 2 * (s & 3)) % 7;
        const int xr = lane >> 3;
        const u32 vx0 = (128 * T1 + 16 * w + xr) * 896 + 16 * ((lane & 7) ^ (xr >> 1));
        const u32 vx1 = (vx0 ^ 64) + 8 * 896;
        const int sl = (lane >> 2) & 7;
        const u32 vw = (32 * (w >> 2) + 16 * ((w >> 1) & 1) + 8 * (sl >> 2) + 4 * (w & 1) + (sl & 3)) * 2688 + blk * 192 + 64 * (lane >> 5) + 16 * ((lane & 3) ^ (2 * (sl >> 2)));
        constexpr u32 SLT[4] = {0, 28672, 114688, 57344};
#define SLOT(r) SLT[(r) % (LA + 1)]
#define KR(r) ((r) + rot >= 7 ? (r) + rot - 7 : (r) + rot)
#define P1_X(r) do { dma(SLOT(r) + 2048 * w, vx0, sx, KR(r) * 128); dma(SLOT(r) + 2048 * w + 1024, vx1, sx, KR(r) * 128); } while (0)
#define P1_W(r) do { dma(SLOT(r) + 16384 + 1536 * w, vw, sw, KR(r) * (64 * 2688)); dma_lo32(SLOT(r) + 16384 + 1536 * w + 1024, vw, sw, KR(r) * (64 * 2688) + 128); } while (0)
#pragma unroll
        for (int r = 0; r < LA; r++) { P1_X(r); P1_W(r); }
        PREWARM();
        STAMPG(1, 1);
        u64 ep = *(const CAS u64*)(ws + WS_EPOCH);
        const int tl0 = 2 * wl + hf;
        const u32 xb = 1024 * (i >> 3) + 128 * (i & 7) + 16 * (g ^ ((i >> 1) & 7));
        const u32 xa00 = 2048 * tl0 + xb, xa01 = xa00 ^ 64, xa10 = xa00 ^ 2048, xa11 = xa00 ^ (2048 + 64);
        const u32 wb = 16384 + 3072 * (g >> 1) + 64 * (4 * (g & 1) + q) + 8 * p;
        int ct[3]; u32 wa[3];
#pragma unroll
        for (int j = 0; j < 3; j++) { ct[j] = 3 * hf + (j + wl) % 3; wa[j] = wb + 32 * ((ct[j] & 1) ^ (g & 1)) + 512 * (ct[j] >> 1); }
        f32x4 acc[2][3], cs = {0, 0, 0, 0};
#pragma unroll
        for (int j = 0; j < 3; j++) { acc[0][j] = (f32x4){0, 0, 0, 0}; acc[1][j] = (f32x4){0, 0, 0, 0}; }
        float s1 = 0.f, s2 = 0.f;
        u32x4 xp[2];
#pragma unroll
        for (int r = 0; r < 7; r++) {
            constexpr int dummy = 0; (void)dummy;
            const int last = r + LA - 1 < 6 ? r + LA - 1 : 6;
            const int nyoung = 4 * (last - r) + (r < LA ? 1 : 0) + (r > 7 - LA ? 7 : 0);
            WAIT_BAR(nyoung);
            STAMPG(1, 2 + r);
            if (r == 0) asm volatile("" :: "v"(cpf));
            const u32 sb = SLOT(r);
            u32x4 xf[2][2], wf[2][3];
            xf[0][0] = lds128(sb + xa00); wf[0][0] = trfrag(sb + wa[0], sb + wa[0] + 1536); xf[0][1] = lds128(sb + xa10);
            wf[0][1] = trfrag(sb + wa[1], sb + wa[1] + 1536); wf[0][2] = trfrag(sb + wa[2], sb + wa[2] + 1536);
            xf[1][0] = lds128(sb + xa01); wf[1][0] = trfrag(sb + wa[0] + 6144, sb + wa[0] + 7680); xf[1][1] = lds128(sb + xa11);
            wf[1][1] = trfrag(sb + wa[1] + 6144, sb + wa[1] + 7680); wf[1][2] = trfrag(sb + wa[2] + 6144, sb + wa[2] + 7680);
            SB();
            if (r + LA < 7) P1_X(r + LA);
            if (r > 0) {
#pragma unroll
                for (int c = 0; c < 2; c++)
#pragma unroll
                    for (int j = 0; j < 4; j++) { s1 = dot2(xp[c][j], 0x3f803f80u, s1); s2 = dot2(xp[c][j], xp[c][j], s2); }
            }
            SB();
#pragma unroll
            for (int cl = 0; cl < 2; cl++) {
                acc[0][0] = mfma(wf[cl][0], xf[cl][0], acc[0][0]); acc[1][0] = mfma(wf[cl][0], xf[cl][1], acc[1][0]);
                cs = mfma(wf[cl][0], ones, cs);
                acc[0][1] = mfma(wf[cl][1], xf[cl][0], acc[0][1]); acc[1][1] = mfma(wf[cl][1], xf[cl][1], acc[1][1]);
                acc[0][2] = mfma(wf[cl][2], xf[cl][0], acc[0][2]); acc[1][2] = mfma(wf[cl][2], xf[cl][1], acc[1][2]);
                SB();
                if (cl == 0 && r + LA < 7) P1_W(r + LA);
                if (r == 7 - LA) {
#pragma unroll
                    for (int k = 4 * cl; k < (cl ? 7 : 4); k++) dma(L_WO + (7 * w + k) * 1024, vwo, so, (7 * w + k) * 8 * 896);
                }
                SB();
            }
            xp[0] = xf[0][0]; xp[1] = xf[1][0];
        }
#pragma unroll
        for (int c = 0; c < 2; c++)
#pragma unroll
            for (int j = 0; j < 4; j++) { s1 = dot2(xp[c][j], 0x3f803f80u, s1); s2 = dot2(xp[c][j], xp[c][j], s2); }
        if (i == 0) ldsf4w(L_SCR + 64 * ct[0] + 16 * g, cs);
        s1 = xsum(s1); s2 = xsum(s2);
        const float mean = s1 * (1.f / 448.f);
        const float var = fmaxf(s2 * (1.f / 448.f) - mean * mean, 0.f);
        f32x2 rm0; rm0[0] = __builtin_amdgcn_rsqf(var + 1e-5f); rm0[1] = -mean * rm0[0];
        if (g == 0) ldsf2w(L_SCR + 512 + 128 * tl0 + 8 * i, rm0);
        STAMPG(1, 9);
        BAR();
        STAMPG(1, 10);
        const f32x2 rm1 = ldsf2(L_SCR + 512 + 128 * (tl0 ^ 1) + 8 * i);
        const u32 qo = WS_QKV + (((8 * T1 + (tl0 ^ (g & 1))) * 168 + 12 * blk + (g >> 1)) * 16 + i) * 16;
#pragma unroll
        for (int j = 0; j < 3; j++) {
            const f32x4 c = ldsf4(L_SCR + 64 * ct[j] + 16 * g);
            const f32x4 v0 = acc[0][j] * rm0[0] + c * rm0[1], v1 = acc[1][j] * rm1[0] + c * rm1[1];
            auto a = __builtin_amdgcn_permlane16_swap(pk2(v0[0], v0[1]), pk2(v1[0], v1[1]), false, false);
            auto bb = __builtin_amdgcn_permlane16_swap(pk2(v0[2], v0[3]), pk2(v1[2], v1[3]), false, false);
            u32x4 o = {a[0], bb[0], a[1], bb[1]};
            st_sc1(rws, o, qo, ct[j] * 512);
        }
        tag = ep + 0x9E3779B97F4A7C15ull;
        tag = (tag ^ (tag >> 30)) * 0xBF58476D1CE4E5B9ull; tag = (tag ^ (tag >> 27)) * 0x94D049BB133111EBull; tag ^= tag >> 31;
        STAMP(1);
        STAMPG(1, 11);
        WAIT_BAR(0);
        STAMPG(1, 12);
        if (w == 0) set_flags(rws, WS_FA + (T1 * 14 + blk) * 128, 1, tag);
    } else {
        const u32x4 sd = mksrd(gwd, 1792 * 896);
#pragma unroll
        for (int k = 0; k < 6; k++) dma((8 * k + w) * 1024, lane * 16, sd, fsh * 50176 + (8 * k + w) * 1024);
        dma(48 * 1024, lane * 16, sd, fsh * 50176 + 48 * 1024);
        PREWARM();
        u64 ep = *(const CAS u64*)(ws + WS_EPOCH);
        u32 tv[6];
        {
            const int t = (s - 28) * 512 + threadIdx.x;
            const int i0 = 3135 - t, i1 = i0 - 2048 < 0 ? 0 : i0 - 2048;
            const u32 o0 = (i0 / 7) * 3584 + (i0 % 7) * 128 + (xcd & 3) * 896, o1 = (i1 / 7) * 3584 + (i1 % 7) * 128 + (xcd & 3) * 896;
            asm volatile("global_load_dword %0, %6, %10\n\tglobal_load_dword %1, %7, %10\n\tglobal_load_dword %2, %8, %11\n\tglobal_load_dword %3, %8, %12\n\t"
                         "global_load_dword %4, %9, %11\n\tglobal_load_dword %5, %9, %12"
                         : "=&v"(tv[0]), "=&v"(tv[1]), "=&v"(tv[2]), "=&v"(tv[3]), "=&v"(tv[4]), "=&v"(tv[5])
                         : "v"(i0 * 128), "v"(i1 * 128), "v"(o0), "v"(o1), "s"(gwo), "s"(gwg), "s"(gwu) : "memory");
        }
        WAIT_BAR(7);
        asm volatile("" :: "v"(cpf));
        const int a4 = fsh >> 2, pp = fsh & 3;
        const u32 ra = (4 * g + q) * 896 + 8 * p;
        const u32 rb = (g < 2 ? 32 + 4 * g + q : 48 + q) * 896 + 8 * p, rb2 = rb + (g < 2 ? 8 : 4) * 896;
        const u32 wo2 = g < 2 ? (4 + (pp >> 1)) * 4096 + (pp & 1) * 512 + lane * 16 : 6 * 4096 + pp * 256 + i * 16;
#pragma unroll
        for (int k = 0; k < 4; k++) {
            const int nt = w + 8 * k;
            if (nt < 28) {
                const u32 dst = WS_WT + (((nt >> 2) * 8 + a4) * 28 + (nt & 3)) * 1024;
                st_sc1(rws, trfrag(ra + 32 * nt, ra + 32 * nt + 16 * 896), pp * 4096 + lane * 16, dst);
                u32x4 v = trfrag(rb + 32 * nt, rb2 + 32 * nt);
                if (g < 3) st_sc1(rws, v, wo2, dst);
            }
        }
        tag = ep + 0x9E3779B97F4A7C15ull;
        tag = (tag ^ (tag >> 30)) * 0xBF58476D1CE4E5B9ull; tag = (tag ^ (tag >> 27)) * 0x94D049BB133111EBull; tag ^= tag >> 31;
        STAMP(1);
        WAIT_BAR(0);
        asm volatile("" :: "v"(tv[0]), "v"(tv[1]), "v"(tv[2]), "v"(tv[3]), "v"(tv[4]), "v"(tv[5]));
        if (w == 0) set_flags(rws, WS_FW + fsh * 128, 1, tag);
    }
    asm volatile("" : "+s"(tag));
    STAMP(2);
    const int gl = lane < 56 ? lane : 55;
    const char* gup = ((gl % 14) < 7 ? gwg + (gl % 14) * 16 : gwu + (gl % 14 - 7) * 16) + (4 * w + gl / 14) * 3584 + fs * 112;
#pragma unroll
    for (int a = 8; a < 14; a++) dma_g56(L_GUHI + (32 * (a - 8) + 4 * w) * 224, gup + a * (32 * 3584));

    {
        const int c0 = (7 * h) >> 1;
        {
            const int role = w < 3 ? 1 : w < 6 ? 2 : 0;
            const int Tp = role == 0 ? T : T - 2 + (w < 3 ? w : w - 3);
            const int clo = role == 1 ? 448 + 8 * c0 : 448 * role + 28 * h;
            const u32 dst = w < 6 ? 8192 * w : L_Q + 4096 * (w - 6);
            if (Tp >= 0) {
                const int col = clo + (lane & 1) * (role == 1 ? 31 : 27);
                const u32 vo = (16 * g + (role == 2 ? i ^ (8 * (g & 1)) : i)) * 16;
                const u32 src = WS_QKV + ((8 * Tp + (w == 7 ? 4 : 0)) * 168 + 56 * role + c0) * 256;
                wait_flags(rws, WS_FA + (Tp * 14 + col / 96) * 128, 3, tag);
                STAMP(3);
#pragma unroll
                for (int n = 0; n < 4; n++) dma_sc1(dst + 1024 * n, vo, sws, src + n * (168 * 256));
                if (w < 6) {
#pragma unroll
                    for (int n = 4; n < 8; n++) dma_sc1(dst + 1024 * n, vo, sws, src + n * (168 * 256));
                }
            } else if (role == 2) {
#pragma unroll
                for (int n = 0; n < 8; n++) lds128w(dst + 1024 * n + lane * 16, (u32x4){0, 0, 0, 0});
            }
        }
        f32x4 m0, m16;
#pragma unroll
        for (int r = 0; r < 4; r++) { m0[r] = 4 * g + r > i ? 0.f : -1e30f; m16[r] = 4 * g + r > i ? -1e30f : 0.f; }
        const bool fg = (h & 1) ? (g == 0) : (g == 3);
        const u32 zlo = (fg && (h & 1)) ? 0u : ~0u, zhi = (fg && !(h & 1)) ? 0u : ~0u;
        const u32 kb = L_K + w * 1024 + lane * 16;
        const int bo = 8 * (h & 1) + 8 * p;
        const u32 vb = L_V + w * 1024 + (bo >> 4) * 256 + (((4 * g + q) ^ (8 * ((bo >> 4) & 1))) * 16) + (bo & 8);
        WAIT_BAR(0);
        STAMP(4);
        u32x4 qf = lds128(L_Q + w * 1024 + lane * 16);
        qf[0] &= zlo; qf[1] &= zlo; qf[2] &= zhi; qf[3] &= zhi;
        f32x4 sc[17];
#pragma unroll
        for (int n = 0; n < 17; n++) sc[n] = mfma(lds128(kb + n * 1024), qf, n == 0 ? m0 : n == 16 ? m16 : (f32x4){0, 0, 0, 0});
        const int nneg = 16 - (8 * T + w);
        if (nneg > 0) {
#pragma unroll
            for (int n = 0; n < 16; n++) if (n < nneg) sc[n] = (f32x4){-1e30f, -1e30f, -1e30f, -1e30f};
        }
        float m = -3e38f;
#pragma unroll
        for (int n = 0; n < 17; n++) { m = fmaxf(fmaxf(m, sc[n][0]), sc[n][1]); m = fmaxf(fmaxf(m, sc[n][2]), sc[n][3]); }
        constexpr float CQ = 0.18898223650461362f * 1.4426950408889634f;
        const float mc = xmax(m) * -CQ;
        f32x4 o0 = {0, 0, 0, 0}, o1 = {0, 0, 0, 0}, ol = {0, 0, 0, 0};
#pragma unroll
        for (int j = 0; j < 9; j++) {
            const f32x4 a = sc[2 * j] * CQ + mc;
            u32x4 pb = {pk2(__builtin_amdgcn_exp2f(a[0]), __builtin_amdgcn_exp2f(a[1])), pk2(__builtin_amdgcn_exp2f(a[2]), __builtin_amdgcn_exp2f(a[3])), 0u, 0u};
            if (j < 8) {
                const f32x4 c = sc[2 * j + 1] * CQ + mc;
                pb[2] = pk2(__builtin_amdgcn_exp2f(c[0]), __builtin_amdgcn_exp2f(c[1])); pb[3] = pk2(__builtin_amdgcn_exp2f(c[2]), __builtin_amdgcn_exp2f(c[3]));
            }
            const u32 va = vb + j * 2048, vh = j < 8 ? va + 1024 : va;
            o0 = mfma(trfrag(va, vh), pb, o0);
            o1 = mfma(trfrag(va + 512, vh + 512), pb, o1);
            ol = mfma(ones, pb, ol);
        }
        const float rl = __builtin_amdgcn_rcpf(ol[0]);
        o0 *= rl; o1 *= rl;
        u32x4 of = {pk2(o0[0], o0[1]), pk2(o0[2], o0[3]), g == 3 ? 0u : pk2(o1[0], o1[1]), g == 3 ? 0u : pk2(o1[2], o1[3])};
        st_sc1(rws, of, WS_O + lane * 16, ((8 * T + w) * 16 + h) * 1024);
        STAMP(5);
    }

    u32x4 hres = {0, 0, 0, 0};
    u32x4 wd[28];
    const u32 wts = WS_WT + (nb * 8 + w) * 28 * 1024;
#define LDWT(f) wd[f] = ld_sc1(rws, lane * 16, wts + (((f) & ~3) + (((f) & 3) ^ (2 * hf))) * 1024)
    const u32 cgb = (4 * g + q) * 224 + 8 * p;
    const u32 cmb = (4 * g + q) * 224 + (p < 2 ? 96 : 192) + 8 * p;
    if (p3act) {
        u32x4 wf[8][4];
        const int g1 = g == 3 ? 1 : g;
        const u32 fe = 2 * (g & 1) + (q >> 1);
        const u32 r0 = L_WO + (224 * hf + 4 * g + q) * 128 + 8 * p, r1 = L_WO + (224 * hf + 16 + 4 * g1 + q) * 128 + 8 * p;
#pragma unroll
        for (int hh = 0; hh < 8; hh++)
#pragma unroll
            for (int t = 0; t < 4; t++) {
                const u32 sw32 = 32 * ((t ^ (2 * (hh & 1))) ^ fe);
                wf[hh][t] = trfrag(r0 + sw32 + hh * 28 * 128, r1 + sw32 + hh * 28 * 128);
            }
        wait_flags(rws, WS_FW + (4 * w + lane) * 128, 15, tag);
        set_flags(rws, WS_FB + ((8 * T + w) * 16 + h) * 128, 1, tag);
        BAR();
        STAMP(6);
#pragma unroll
        for (int a = 0; a < 8; a++) dma_g56((32 * a + 4 * w) * 224, gup + a * (32 * 3584));
        const rsrc_t rx = __builtin_amdgcn_make_buffer_rsrc((void*)gx, 0, 2048 * 896, 0x00020000);
        const u32 xo = (64 * tb + 16 * wl + i) * 896 + (64 * nb + 32 * hf + 4 * g) * 2;
        u32x2 xr0 = __builtin_amdgcn_raw_buffer_load_b64(rx, xo, 0, 0), xr1 = __builtin_amdgcn_raw_buffer_load_b64(rx, xo + 32, 0, 0);
#pragma unroll
        for (int f = 0; f < NWT1; f++) LDWT(f);
        wait_flags(rws, WS_FB + ((4 * tb + wl) * 16 + 8 * hf + lane) * 128, 255, tag);
        STAMP(7);
        u32x4 of[8];
#pragma unroll
        for (int hh = 0; hh < 8; hh++) of[hh] = ld_sc1(rws, WS_O + lane * 16, ((4 * tb + wl) * 16 + 8 * hf + hh) * 1024);
        SB();
        f32x4 acc[4];
#pragma unroll
        for (int t = 0; t < 4; t++) acc[t] = (f32x4){0, 0, 0, 0};
#pragma unroll
        for (int hh = 0; hh < 8; hh++)
#pragma unroll
            for (int t = 0; t < 4; t++) acc[t] = mfma(wf[hh][t], of[hh], acc[t]);
        ldsf4w(L_WO + (w ^ 4) * 2048 + lane * 16, hf ? acc[0] : acc[2]);
        ldsf4w(L_WO + (w ^ 4) * 2048 + 1024 + lane * 16, hf ? acc[1] : acc[3]);
        BAR();
        f32x4 h0 = (hf ? acc[2] : acc[0]) + ldsf4(L_WO + w * 2048 + lane * 16);
        f32x4 h1 = (hf ? acc[3] : acc[1]) + ldsf4(L_WO + w * 2048 + 1024 + lane * 16);
        hres[0] = pk2(h0[0] + bflo(xr0[0]), h0[1] + bfhi(xr0[0])); hres[1] = pk2(h0[2] + bflo(xr0[1]), h0[3] + bfhi(xr0[1]));
        hres[2] = pk2(h1[0] + bflo(xr1[0]), h1[1] + bfhi(xr1[0])); hres[3] = pk2(h1[2] + bflo(xr1[1]), h1[3] + bfhi(xr1[1]));
        st_sc1(rws, hres, WS_H + lane * 16, ((4 * tb + wl) * 14 + 2 * nb + hf) * 1024);
        {
            float s1 = 0.f, s2 = 0.f;
#pragma unroll
            for (int j = 0; j < 4; j++) { s1 = dot2(hres[j], 0x3f803f80u, s1); s2 = dot2(hres[j], hres[j], s2); }
            s1 = xsum(s1); s2 = xsum(s2);
            u32x4 v = {asu(s1), asu(s2), (u32)__builtin_amdgcn_mov_dpp((int)asu(s1), 0xB1, 0xf, 0xf, false), (u32)__builtin_amdgcn_mov_dpp((int)asu(s2), 0xB1, 0xf, 0xf, false)};
            if ((lane & 49) == 0) st_sc1(rws, v, WS_ST + lane * 8, ((4 * tb + wl) * 14 + 2 * nb + hf) * 128);
        }
        STAMP(8);
        asm volatile("" ::: "memory");
#pragma unroll
        for (int f = NWT1; f < NWT1 + NWT2; f++) LDWT(f);
    } else {
        WAIT_VM(0);
        set_flags(rws, WS_FB + ((8 * T + w) * 16 + h) * 128, 1, tag);
        BAR();
#pragma unroll
        for (int a = 0; a < 8; a++) dma_g56((32 * a + 4 * w) * 224, gup + a * (32 * 3584));
        if (b == 255 && w == 0) {
#pragma unroll
            for (int k = 0; k < 4; k++) wait_flags(rws, WS_FB + ((4 * k + g) * 128 + i) * 128, ~0ull, tag);
            set_flags(rws, WS_EPOCH, 1, tag);
        }
        WAIT_BAR(0);
    }

    {
        {
            const u32 cb = w >= 6 ? cmb : cgb + (w < 3 ? 32 * w : 112 + 32 * (w - 3));
            f32x4 cs = {0, 0, 0, 0};
#pragma unroll
            for (int c = 0; c < 14; c++) { const u32 ro = (c < 8 ? 0 : L_GUHI - 57344) + c * 7168; cs = mfma(trfrag(cb + ro, cb + ro + 3584), ones, cs); }
            if (w < 7 && i == 0) ldsf4w(L_SCR + 64 * w + 16 * g, cs);
        }
        WAIT_BAR(NWT2);
        if (p3act) {
            if (w == 0) set_flags(rws, WS_FC + ((tb * 7 + nb) * 2 + lane) * 128, 3, tag);
#pragma unroll
            for (int f = NWT1 + NWT2; f < 28; f++) LDWT(f);
        }
        const int tb4 = 4 * tg + wl, tt0 = 4 * tb4 + 2 * hf;
        wait_flags(rws, WS_FC + ((tb4 * 7 + lane) * 2 + hf) * 128, 127, tag);
        STAMP(9);
        if (hf == 0) __builtin_amdgcn_s_setprio(2);
        u32x4 hw[HW][2];
        const u32 hs = WS_H + tt0 * 14 * 1024;
        hw[0][0] = ld_sc1(rws, lane * 16, hs); hw[0][1] = ld_sc1(rws, lane * 16, hs + 14 * 1024);
        u32x2 sv[2][4];
        const u32 svo = g * 128 + i * 8;
#pragma unroll
        for (int c = 1; c < HW; c++) { hw[c][0] = ld_sc1(rws, lane * 16, hs + c * 1024); hw[c][1] = ld_sc1(rws, lane * 16, hs + (14 + c) * 1024); }
#pragma unroll
        for (int t = 0; t < 2; t++)
#pragma unroll
            for (int j = 0; j < 4; j++) sv[t][j] = ld2_sc1(rws, j == 3 ? (svo & 255) + 1536 : svo + 512 * j, WS_ST + (tt0 + t) * 14 * 128);
        f32x4 acc[2][7];
#pragma unroll
        for (int t = 0; t < 2; t++)
#pragma unroll
            for (int n = 0; n < 7; n++) acc[t][n] = (f32x4){0, 0, 0, 0};
        u32x4 wf[PD + 1];
#define GUA(j) (((j) % 7 == 6 ? cmb : cgb + ((j) % 7 < 3 ? 32 * ((j) % 7) : 112 + 32 * ((j) % 7 - 3))) + ((j) / 7 < 8 ? 0 : L_GUHI - 57344) + ((j) / 7) * 7168)
#pragma unroll
        for (int j = 0; j < PD; j++) wf[j] = trfrag(GUA(j), GUA(j) + 3584);
        float mean[2], kx[2], iv[2];
#pragma unroll
        for (int j = 0; j < 98; j++) {
            const int c = j / 7, n = j % 7;
            if (j + PD < 98) wf[(j + PD) % (PD + 1)] = trfrag(GUA(j + PD), GUA(j + PD) + 3584);
            SB();
            acc[0][n] = mfma(wf[j % (PD + 1)], hw[c % HW][0], acc[0][n]);
            acc[1][n] = mfma(wf[j % (PD + 1)], hw[c % HW][1], acc[1][n]);
            if (n == 6 && c + HW < 14) { hw[c % HW][0] = ld_sc1(rws, lane * 16, hs + (c + HW) * 1024); hw[c % HW][1] = ld_sc1(rws, lane * 16, hs + (14 + c + HW) * 1024); }
            if (j == 7 * HW + 1 || j == 7 * HW + 3) {
                const int t = j == 7 * HW + 3;
                float a1 = asf(sv[t][0][0]) + asf(sv[t][1][0]) + asf(sv[t][2][0]) + (g < 2 ? asf(sv[t][3][0]) : 0.f);
                float a2 = asf(sv[t][0][1]) + asf(sv[t][1][1]) + asf(sv[t][2][1]) + (g < 2 ? asf(sv[t][3][1]) : 0.f);
                a1 = xsum(a1); a2 = xsum(a2);
                mean[t] = a1 * (-1.f / 448.f);
                iv[t] = fmaxf(a2 * (1.f / 448.f) - mean[t] * mean[t], 0.f) + 1e-5f;
                kx[t] = __builtin_amdgcn_rsqf(iv[t]) * -1.4426950408889634f;
            }
            SB();
        }
        STAMP(10);
        u32 pa[2][3][2]; f32x4 mm[2];
#pragma unroll
        for (int t = 0; t < 2; t++) {
#pragma unroll
            for (int n = 0; n < 3; n++) {
                const f32x4 ga = ldsf4(L_SCR + 64 * n + 16 * g) * mean[t] + acc[t][n];
                const f32x4 ua = ldsf4(L_SCR + 64 * (3 + n) + 16 * g) * mean[t] + acc[t][3 + n];
                const f32x4 ex = ga * kx[t];
                f32x4 e = {__builtin_amdgcn_exp2f(ex[0]), __builtin_amdgcn_exp2f(ex[1]), __builtin_amdgcn_exp2f(ex[2]), __builtin_amdgcn_exp2f(ex[3])};
                e = e * iv[t] + iv[t];
                const f32x4 rc = {__builtin_amdgcn_rcpf(e[0]), __builtin_amdgcn_rcpf(e[1]), __builtin_amdgcn_rcpf(e[2]), __builtin_amdgcn_rcpf(e[3])};
                const f32x4 av = ga * rc * ua;
                pa[t][n][0] = pk2(av[0], av[1]); pa[t][n][1] = pk2(av[2], av[3]);
            }
            mm[t] = ldsf4(L_SCR + 64 * 6 + 16 * g) * mean[t] + acc[t][6];
        }
        const int a4 = fs >> 2, pp = fs & 3;
#pragma unroll
        for (int t = 0; t < 2; t++) {
            u32x4 v = {pa[t][0][0], pa[t][0][1], pa[t][1][0], pa[t][1][1]};
            st_sc1(rws, v, WS_ACT + lane * 16, ((tt0 + t) * 56 + 7 * a4 + pp) * 1024);
        }
        {
            auto x0 = __builtin_amdgcn_permlane32_swap(pa[0][2][0], pa[1][2][0], false, false);
            auto x1 = __builtin_amdgcn_permlane32_swap(pa[0][2][1], pa[1][2][1], false, false);
            u32x4 v = {x0[0], x1[0], x0[1], x1[1]};
            st_sc1(rws, v, WS_ACT + (g >> 1) * (56 * 1024) + ((g & 1) * 16 + i) * 16, (tt0 * 56 + 7 * a4 + 4 + (pp >> 1)) * 1024 + (pp & 1) * 512);
        }
        {
            const float kq = g < 2 ? kx[0] : kx[1], iq = g < 2 ? iv[0] : iv[1];
            f32x4 av;
#pragma unroll
            for (int r = 0; r < 4; r++) {
                auto sw2 = __builtin_amdgcn_permlane32_swap(asu(mm[0][r]), asu(mm[1][r]), false, false);
                const float ga = asf(sw2[0]), ua = asf(sw2[1]);
                av[r] = ga * __builtin_amdgcn_rcpf(__builtin_amdgcn_exp2f(ga * kq) * iq + iq) * ua;
            }
            const u32 m0 = pk2(av[0], av[1]), m1 = pk2(av[2], av[3]);
            auto y0 = __builtin_amdgcn_permlane16_swap(m0, m0, false, false);
            auto y1 = __builtin_amdgcn_permlane16_swap(m1, m1, false, false);
            u32x4 v = {y0[0], y1[0], y0[1], y1[1]};
            if ((g & 1) == 0) st_sc1(rws, v, WS_ACT + (g >> 1) * (56 * 1024) + i * 16, (tt0 * 56 + 7 * a4 + 6) * 1024 + pp * 256);
        }
        STAMP(11);
        WAIT_VM(0);
        set_flags(rws, WS_FD + ((tb4 * 2 + hf) * 32 + fs) * 128, 1, tag);
        __builtin_amdgcn_s_setprio(0);
    }

    if (p3act) {
#pragma unroll
        for (int hb = 0; hb < 2; hb++) {
            wait_flags(rws, WS_FD + ((tb * 2 + hb) * 32 + 4 * w + lane) * 128, 15, tag);
            STAMP(12 + hb);
            u32x4 af[2][7];
#pragma unroll
            for (int tl = 0; tl < 2; tl++)
#pragma unroll
                for (int k = 0; k < 7; k++) af[tl][k] = ld_sc1(rws, WS_ACT + lane * 16, ((4 * tb + 2 * hb + ((wl & 1) ^ tl ^ 1)) * 56 + 7 * w + k) * 1024);
            SB();
            const bool own = (wl >> 1) == hb;
            const u32 pbase = (hb ? 0 : L_WO) + lane * 16;
            f32x4 acc[4];
#pragma unroll
            for (int tl = 0; tl < 2; tl++) {
#pragma unroll
                for (int t = 0; t < 4; t++) acc[t] = (f32x4){0, 0, 0, 0};
#pragma unroll
                for (int k = 0; k < 7; k++)
#pragma unroll
                    for (int t = 0; t < 4; t++) acc[3 - t] = mfma(wd[4 * k + 3 - t], af[tl][k], acc[3 - t]);
#pragma unroll
                for (int t = 3; t >= 0; t--) {
                    const int tq = (wl & 1) ^ tl ^ 1, n = t ^ (2 * hf);
                    const int wo = 2 * hb + tq + 4 * (n >> 1);
                    if (tl == 0 || t >= 2 || !own) ldsf4w(pbase + ((tq * 4 + n) * 7 + (w > wo ? w - 1 : w)) * 1024, acc[t]);
                }
            }
            BAR();
            if (own) {
                f32x4 r0 = acc[0], r1 = acc[1];
                const u32 rb = pbase + ((wl & 1) * 4 + 2 * hf) * 7 * 1024;
#pragma unroll
                for (int k = 0; k < 7; k++) { r0 += ldsf4(rb + k * 1024); r1 += ldsf4(rb + (7 + k) * 1024); }
                u32 a0 = pk2(r0[0] + bflo(hres[0]), r0[1] + bfhi(hres[0])), a1 = pk2(r0[2] + bflo(hres[1]), r0[3] + bfhi(hres[1]));
                u32 b0 = pk2(r1[0] + bflo(hres[2]), r1[1] + bfhi(hres[2])), b1 = pk2(r1[2] + bflo(hres[3]), r1[3] + bfhi(hres[3]));
                auto x0 = __builtin_amdgcn_permlane16_swap(a0, b0, false, false);
                auto x1 = __builtin_amdgcn_permlane16_swap(a1, b1, false, false);
                u32x4 v = {x0[0], x1[0], x0[1], x1[1]};
                *(u32x4*)(gout + (size_t)(64 * tb + 16 * wl + i) * 896 + (64 * nb + 32 * hf + 8 * (g >> 1) + 16 * (g & 1)) * 2) = v;
            }
        }
        STAMP(14);
    }
    asm volatile(".Lfz_end:" ::: "memory");
}

extern "C" void kernel_launch(void* const* d_in, const int* in_sizes, int n_in, void* d_out, int out_size, void* d_ws, size_t ws_size,
                              hipStream_t stream) {
    fused<<<256, 512, 0, stream>>>((const char*)d_in[0], (const char*)d_in[1], (const char*)d_in[2], (const char*)d_in[4],
                                   (const char*)d_in[5], (const char*)d_in[6], (char*)d_out, (char*)d_ws);
}
```

```cpp
#include <hip/hip_runtime.h>
#include <stdint.h>

typedef unsigned u32;
typedef unsigned long long u64;
typedef u32 u32x2 __attribute__((ext_vector_type(2)));
typedef u32 u32x4 __attribute__((ext_vector_type(4)));
typedef float f32x2 __attribute__((ext_vector_type(2)));
typedef float f32x4 __attribute__((ext_vector_type(4)));
typedef __bf16 bf16x2 __attribute__((ext_vector_type(2)));
typedef __bf16 bf16x8 __attribute__((ext_vector_type(8)));
typedef short s16x4 __attribute__((ext_vector_type(4)));
typedef short s16x8 __attribute__((ext_vector_type(8)));
#define LAS __attribute__((address_space(3)))
#define CAS __attribute__((address_space(4)))
#define DEVI __device__ __forceinline__

constexpr u32 WS_EPOCH = 0;
constexpr u32 WS_FA = 4096;
constexpr u32 WS_FC = 32768;
constexpr u32 WS_FW = 90112;
constexpr u32 WS_FB = 131072;
constexpr u32 WS_FD = 393216;
constexpr u32 WS_ST = 655360;
constexpr u32 WS_QKV = 1048576;
constexpr u32 WS_O = WS_QKV + 128 * 168 * 256;
constexpr u32 WS_H = WS_O + 128 * 16 * 1024;
constexpr u32 WS_ACT = WS_H + 128 * 14 * 1024;
constexpr u32 WS_WT = WS_ACT + 128 * 56 * 1024;
constexpr u32 WS_END = WS_WT + 7 * 8 * 28 * 1024;
constexpr u32 WS_STAMP = WS_END;

constexpr u32 L_WO = 57344;
constexpr u32 L_GUHI = 114688;
constexpr u32 L_SCR = 157696;
constexpr u32 L_K = 114688, L_V = 139264;

__shared__ __attribute__((aligned(1024))) char smem[163840];

DEVI f32x4 mfma(u32x4 a, u32x4 b, f32x4 c) {
    return __builtin_amdgcn_mfma_f32_16x16x32_bf16(__builtin_bit_cast(bf16x8, a), __builtin_bit_cast(bf16x8, b), c, 0, 0, 0);
}
DEVI f32x4 mfma16(u32x2 a, u32x2 b, f32x4 c) {
    return __builtin_amdgcn_mfma_f32_16x16x16bf16_1k(__builtin_bit_cast(s16x4, a), __builtin_bit_cast(s16x4, b), c, 0, 0, 0);
}
DEVI u32x2 trhalf(u32 a0) { return __builtin_bit_cast(u32x2, __builtin_amdgcn_ds_read_tr16_b64_v4i16((LAS s16x4*)(smem + a0))); }
DEVI u32x4 trfrag(u32 a0, u32 a1) {
    s16x4 lo = __builtin_amdgcn_ds_read_tr16_b64_v4i16((LAS s16x4*)(smem + a0));
    s16x4 hi = __builtin_amdgcn_ds_read_tr16_b64_v4i16((LAS s16x4*)(smem + a1));
    return __builtin_bit_cast(u32x4, __builtin_shufflevector(lo, hi, 0, 1, 2, 3, 4, 5, 6, 7));
}
DEVI u32x4 lds128(u32 a) { return *(LAS u32x4*)(smem + a); }
DEVI void lds128w(u32 a, u32x4 v) { *(LAS u32x4*)(smem + a) = v; }
DEVI f32x4 ldsf4(u32 a) { return *(LAS f32x4*)(smem + a); }
DEVI void ldsf4w(u32 a, f32x4 v) { *(LAS f32x4*)(smem + a) = v; }
DEVI f32x2 ldsf2(u32 a) { return *(LAS f32x2*)(smem + a); }
DEVI void ldsf2w(u32 a, f32x2 v) { *(LAS f32x2*)(smem + a) = v; }
DEVI u32 pk2(float a, float b) { f32x2 f = {a, b}; return __builtin_bit_cast(u32, __builtin_convertvector(f, bf16x2)); }
DEVI float dot2(u32 a, u32 b, float c) { return __builtin_amdgcn_fdot2_f32_bf16(__builtin_bit_cast(bf16x2, a), __builtin_bit_cast(bf16x2, b), c, false); }
DEVI float bflo(u32 v) { return __builtin_bit_cast(float, v << 16); }
DEVI float bfhi(u32 v) { return __builtin_bit_cast(float, v & 0xffff0000u); }
DEVI float asf(u32 v) { return __builtin_bit_cast(float, v); }
DEVI u32 asu(float v) { return __builtin_bit_cast(u32, v); }
DEVI float xsum(float v) {
    u32 u = asu(v);
    auto s = __builtin_amdgcn_permlane16_swap(u, u, false, false);
    v = asf(s[0]) + asf(s[1]);
    u = asu(v);
    auto t = __builtin_amdgcn_permlane32_swap(u, u, false, false);
    return asf(t[0]) + asf(t[1]);
}
DEVI float xmax(float v) {
    u32 u = asu(v);
    auto s = __builtin_amdgcn_permlane16_swap(u, u, false, false);
    v = fmaxf(fmaxf(asf(s[0]), asf(s[1])), -3e38f);
    u = asu(v);
    auto t = __builtin_amdgcn_permlane32_swap(u, u, false, false);
    return fmaxf(fmaxf(asf(t[0]), asf(t[1])), -3e38f);
}
DEVI float mx3(float a, float b, float c) { return fmaxf(fmaxf(a, b), c); }
DEVI u32x4 mksrd(const void* p, u32 bytes) {
    u64 a = (u64)p;
    u32x4 r = {(u32)a, (u32)(a >> 32) & 0xffffu, bytes, 0x00020000u};
    return r;
}
DEVI void dma(u32 lds, u32 voff, u32x4 srd, u32 soff) {
    asm volatile("s_mov_b32 m0, %0\n\ts_nop 0\n\tbuffer_load_dwordx4 %1, %2, %3 offen lds" :: "s"(lds), "v"(voff), "s"(srd), "s"(soff) : "memory");
}
DEVI void dma_sc1(u32 lds, u32 voff, u32x4 srd, u32 soff) {
    asm volatile("s_mov_b32 m0, %0\n\ts_nop 0\n\tbuffer_load_dwordx4 %1, %2, %3 offen sc1 lds" :: "s"(lds), "v"(voff), "s"(srd), "s"(soff) : "memory");
}
DEVI void dma_lo32(u32 lds, u32 voff, u32x4 srd, u32 soff) {
    asm volatile("s_mov_b32 m0, %0\n\ts_mov_b32 exec_hi, 0\n\tbuffer_load_dwordx4 %1, %2, %3 offen lds\n\ts_mov_b32 exec_hi, -1"
                 :: "s"(lds), "v"(voff), "s"(srd), "s"(soff) : "memory");
}
DEVI void dma_g56(u32 lds, const char* p) {
    asm volatile("s_mov_b32 m0, %0\n\ts_mov_b32 exec_hi, 0xffffff\n\tglobal_load_lds_dwordx4 %1, off\n\ts_mov_b32 exec_hi, -1" :: "s"(lds), "v"(p) : "memory");
}
#define WAIT_BAR(n) asm volatile("s_waitcnt vmcnt(%0) lgkmcnt(0)\n\ts_barrier" :: "n"(n) : "memory")
#define WAIT_VM(n) asm volatile("s_waitcnt vmcnt(%0)" :: "n"(n) : "memory")
#define BAR() asm volatile("s_waitcnt lgkmcnt(0)\n\ts_barrier" ::: "memory")
#define SB() __builtin_amdgcn_sched_barrier(0)

#define STAMP(i) do {} while (0)
#define STCNT(i, v) do {} while (0)
#define STAMPR(i) do {} while (0)
#define STAMPG(grp, i) do {} while (0)
#define SG 0

typedef __amdgpu_buffer_rsrc_t rsrc_t;
DEVI u32x4 ld_sc1(rsrc_t r, u32 voff, u32 soff) { return __builtin_amdgcn_raw_buffer_load_b128(r, voff, soff, 16); }
DEVI u32x2 ld2_sc1(rsrc_t r, u32 voff, u32 soff) { return __builtin_amdgcn_raw_buffer_load_b64(r, voff, soff, 16); }
DEVI void st_sc1(rsrc_t r, u32x4 v, u32 voff, u32 soff) { __builtin_amdgcn_raw_buffer_store_b128(v, r, voff + soff, 0, 16); }

#define APOLL 1
#define ASET 0
#define FST "buffer_store_dwordx2"
#define WAITF(LD) asm volatile( \
        "s_mov_b64 exec, %[am]\n\ts_mov_b32 %[cnt], 20000\n" \
        "1:\n\t" LD "\n\ts_waitcnt vmcnt(0)\n\t" \
        "v_cmp_ne_u64 %[ne], %[v], %[tag]\n\ts_cmp_eq_u64 %[ne], 0\n\ts_cbranch_scc1 2f\n\t" \
        "s_sub_u32 %[cnt], %[cnt], 1\n\ts_cmp_lg_u32 %[cnt], 0\n\ts_cbranch_scc1 1b\n" \
        "2:\n\ts_mov_b64 exec, -1" \
        : [v] "=&v"(v), [ne] "=&s"(ne), [cnt] "=&s"(cnt) \
        : [off] "v"(voff), [r] "s"(r), [am] "s"(am), [tag] "s"(tag) : "memory", "scc")
template <int AT = 0> DEVI void wait_flags(rsrc_t r, u32 voff, u64 am, u64 tag) {
    u32x2 v; u64 ne; u32 cnt;
    if constexpr (AT) WAITF("v_mov_b64 %[v], 0\n\tbuffer_atomic_or_x2 %[v], %[off], %[r], 0 offen sc0 sc1");
    else WAITF("buffer_load_dwordx2 %[v], %[off], %[r], 0 offen sc1");
}
DEVI void set_flags(rsrc_t r, u32 voff, u64 am, u64 tag) {
    asm volatile("s_mov_b64 exec, %[am]\n\t" FST " %[v], %[off], %[r], 0 offen sc1\n\ts_mov_b64 exec, -1"
                 :: [v] "v"(tag), [off] "v"(voff), [r] "s"(r), [am] "s"(am) : "memory");
}

DEVI void flag_then_wait(rsrc_t r, u32 foff, u32 voff, u64 am, u64 tag) {
    u32x2 v; u64 ne; u32 cnt;
    u32x2 tv = {(u32)tag, (u32)(tag >> 32)};
    asm volatile(
        "s_mov_b64 exec, %[am]\n\ts_mov_b32 %[cnt], 20000\n\t"
        "buffer_load_dwordx2 %[v], %[off], %[r], 0 offen sc1\n\ts_waitcnt vmcnt(0)\n\t"
        "s_mov_b64 exec, 1\n\tbuffer_store_dwordx2 %[tv], off, %[r], %[fo] sc1\n\ts_mov_b64 exec, %[am]\n\ts_branch 3f\n"
        "1:\n\tbuffer_load_dwordx2 %[v], %[off], %[r], 0 offen sc1\n\ts_waitcnt vmcnt(0)\n"
        "3:\n\tv_cmp_ne_u64 %[ne], %[v], %[tag]\n\ts_cmp_eq_u64 %[ne], 0\n\ts_cbranch_scc1 2f\n\t"
        "s_sub_u32 %[cnt], %[cnt], 1\n\ts_cmp_lg_u32 %[cnt], 0\n\ts_cbranch_scc1 1b\n"
        "2:\n\ts_mov_b64 exec, -1"
        : [v] "=&v"(v), [ne] "=&s"(ne), [cnt] "=&s"(cnt)
        : [off] "v"(voff), [r] "s"(r), [am] "s"(am), [tag] "s"(tag), [tv] "v"(tv), [fo] "s"(foff) : "memory", "scc");
}
DEVI void set_flag1(rsrc_t r, u32 foff, u64 tag) {
    u32x2 tv = {(u32)tag, (u32)(tag >> 32)};
    asm volatile("s_mov_b64 exec, 1\n\t" FST " %[tv], off, %[r], %[fo] sc1\n\ts_mov_b64 exec, -1" :: [tv] "v"(tv), [r] "s"(r), [fo] "s"(foff) : "memory");
}
DEVI u64 mix(u64 t) {
    t += 0x9E3779B97F4A7C15ull;
    t = (t ^ (t >> 30)) * 0xBF58476D1CE4E5B9ull; t = (t ^ (t >> 27)) * 0x94D049BB133111EBull; return t ^ (t >> 31);
}
template <int V> struct IC { static constexpr int v = V; };
template <int A, int B, class F> DEVI void sfor(F&& f) { if constexpr (A < B) { f(IC<A>{}); sfor<A + 1, B>(f); } }
#define SFOR(j, A, B) sfor<A, B>([&](auto j##_c) __attribute__((always_inline)) { constexpr int j = decltype(j##_c)::v;
#define SEND });
#define PIN(x) asm volatile("" : "+v"(x))

constexpr u32 L_DUMMY = L_SCR + 4096;
DEVI void touch(const void* base, u32 voff) {
    asm volatile("s_mov_b32 m0, %0\n\ts_nop 0\n\tglobal_load_lds_dword %1, %2" :: "n"(L_DUMMY), "v"(voff), "s"(base) : "memory");
}
DEVI u32x4 rld(u32 voff, rsrc_t rs, u32 soff) { u32x4 d; asm volatile("buffer_load_dwordx4 %0, %1, %2, %3 offen sc1" : "=v"(d) : "v"(voff), "s"(rs), "s"(soff) : "memory"); return d; }
template <int N, class V> DEVI void aw(V& a) { asm volatile("s_waitcnt vmcnt(%1)" : "+v"(a) : "n"(N) : "memory"); }
#define PINS(x) asm volatile("" : "+s"(x))
template <class V> DEVI void pins(V& a) { asm volatile("" : "+s"(a)); }
DEVI void dma_r(u32 lds, u32 voff, rsrc_t srd, u32 soff) {
    asm volatile("s_mov_b32 m0, %0\n\ts_nop 0\n\tbuffer_load_dwordx4 %1, %2, %3 offen sc1 lds" :: "s"(lds), "v"(voff), "s"(srd), "s"(soff) : "memory");
}
#define D6S "s_add_u32 m0, m0, 0x400\n\ts_add_u32 %0, %0, 0xa800\n\tbuffer_load_dwordx4 %2, %3, %0 offen sc1 lds\n\t"
DEVI void dma6(u32 lds, u32 voff, rsrc_t srd, u32 soff) {
    asm volatile("s_mov_b32 m0, %1\n\ts_nop 0\n\tbuffer_load_dwordx4 %2, %3, %0 offen sc1 lds\n\t" D6S D6S D6S D6S D6S
                 : "+s"(soff) : "s"(lds), "v"(voff), "s"(srd) : "memory", "scc");
}
template <int N, int BARR> DEVI void publish(rsrc_t r, u32 voff, u32 am, u64 tag) {
    am = __builtin_amdgcn_readfirstlane(am);
    if constexpr (BARR)
        asm volatile("s_mov_b32 exec_lo, %[am]\n\ts_mov_b32 exec_hi, 0\n\ts_waitcnt vmcnt(%[n]) lgkmcnt(0)\n\ts_barrier\n\t" FST " %[v], %[off], %[r], 0 offen sc1\n\ts_mov_b64 exec, -1"
                     :: [v] "v"(tag), [off] "v"(voff), [r] "s"(r), [am] "s"(am), [n] "n"(N) : "memory");
    else
        asm volatile("s_mov_b32 exec_lo, %[am]\n\ts_mov_b32 exec_hi, 0\n\ts_waitcnt vmcnt(%[n])\n\t" FST " %[v], %[off], %[r], 0 offen sc1\n\ts_mov_b64 exec, -1"
                     :: [v] "v"(tag), [off] "v"(voff), [r] "s"(r), [am] "s"(am), [n] "n"(N) : "memory");
}
template <class V> DEVI void pinv(V& a) { asm volatile("" : "+v"(a)); }
template <int AT = 0> DEVI u32x2 peek_issue(rsrc_t r, u32 voff, u64 am) {
    u32x2 v;
    if constexpr (AT)
        asm volatile("v_mov_b64 %[v], 0\n\ts_mov_b64 exec, %[am]\n\tbuffer_atomic_or_x2 %[v], %[off], %[r], 0 offen sc0 sc1\n\ts_mov_b64 exec, -1"
                     : [v] "=&v"(v) : [off] "v"(voff), [r] "s"(r), [am] "s"(am) : "memory");
    else
        asm volatile("s_mov_b64 exec, %[am]\n\tbuffer_load_dwordx2 %[v], %[off], %[r], 0 offen sc1\n\ts_mov_b64 exec, -1"
                     : [v] "=&v"(v) : [off] "v"(voff), [r] "s"(r), [am] "s"(am) : "memory");
    return v;
}
template <int N> DEVI bool peek_test(u32x2 v, u64 am, u64 tag) {
    u64 eq;
    asm volatile("s_waitcnt vmcnt(%[n])\n\tv_cmp_eq_u64 %[eq], %[v], %[tag]" : [eq] "=s"(eq) : [v] "v"(v), [tag] "s"(tag), [n] "n"(N) : "memory");
    return (eq & am) == am;
}
constexpr u32 L_DONE = L_SCR + 2048;
DEVI u32 lds32(u32 a) { return *(volatile LAS u32*)(smem + a); }
DEVI void lds32w(u32 a, u32 v) { *(volatile LAS u32*)(smem + a) = v; }
DEVI void lds_spin(u32 v, u32 a) {
    u32 cnt;
    asm volatile("s_mov_b32 %[cnt], 1000000\n"
                 "1:\n\tv_cmp_ne_u32 vcc, 1, %[v]\n\ts_cbranch_vccz 2f\n\tds_read_b32 %[v], %[a]\n\ts_sub_u32 %[cnt], %[cnt], 1\n\t"
                 "s_waitcnt lgkmcnt(0)\n\ts_cbranch_scc0 1b\n"
                 "2:"
                 : [v] "+v"(v), [cnt] "=&s"(cnt) : [a] "v"(a) : "memory", "scc", "vcc");
}

#define TGU 0
#define TWT 0
#define NG1 0
#define XE 1
#define GH1 4
#define W2L 0
#define PRI 2
#define MC 5
#define PK0 1
#define PK1 8
#define PB 0
#define TW0 10
#define TXL 21
#define TST 17
#define TBR 22
#define TMC (TBR + 3)
#define TE0 29
#define WOA 1
#define GUA 1
#define OLF 1
#define T16 3
#define PINA 0
#define OST 0
#define OSTS "sc0 sc1 nt"
#define GHI 0
#define GPOS 2
#define ROT1 1
#define KWR 1
#define NWT1 4
#define NWT2 2
#define NWT3 6
#define NWT5 6
#define HW 4
#define HW0 4
#define HWJ 0
#define SVC -1
#define PD 3

#if defined(__HIP_DEVICE_COMPILE__) && !defined(PKON)
#define NOPK __attribute__((target("no-packed-fp32-ops")))
#else
#define NOPK
#endif
__global__ void __launch_bounds__(512, 2) NOPK
fused(const char* __restrict__ gx, const char* __restrict__ gwqkv, const char* __restrict__ gwo, const char* __restrict__ gwg,
      const char* __restrict__ gwu, const char* __restrict__ gwd, char* gout, char* ws_arg) {
    u32x4 ka; u64 kw_;
    asm volatile("s_load_dwordx4 %0, %2, 0x0\n\ts_load_dwordx2 %1, %2, 0x38" : "=&s"(ka), "=&s"(kw_) : "s"(__builtin_amdgcn_kernarg_segment_ptr()));
    SB();
    const u32 lane = threadIdx.x & 63;
    const u32 w = __builtin_amdgcn_readfirstlane(threadIdx.x >> 6);
    const u32 b = blockIdx.x, xcd = b & 7, s = b >> 3;
    STAMPR(62);
    STAMP(0);
    {
        u64 pc; u32 len;
        asm volatile("s_getpc_b64 %0\n.Lfz_a:\n\ts_mov_b32 %1, .Lfz_e0-.Lfz_a+512\n\ts_max_u32 %1, %1, .Lfz_e1-.Lfz_a+512\n\ts_max_u32 %1, %1, .Lfz_e2-.Lfz_a+512" : "=s"(pc), "=s"(len) :: "scc");
        const u32 o = (((s & 7) * 8 + w) * 8 + (lane & 7)) * 128;
        touch((const void*)pc, o < len ? o : len);
    }
    const bool p3act = s < 28;
    const u32 blk = 7 * (xcd & 1) + (s >> 2), T1 = 4 * (xcd >> 1) + (s & 3);
    const u32 rot0 = (s >> 2) + 2 * (s & 3), rot = rot0 >= 7 ? rot0 - 7 : rot0;
    const u32 xr = lane >> 3;
    const u32 vx0 = (128 * T1 + 16 * w + xr) * 896 + 16 * ((lane & 7) ^ (xr >> 1));
    const u32 vx1 = (vx0 ^ 64) + 8 * 896;
    const u32 sl = (lane >> 2) & 7;
    const u32 vw = (32 * (w >> 2) + 16 * ((w >> 1) & 1) + 8 * (sl >> 2) + 4 * (w & 1) + (sl & 3)) * 2688 + blk * 192 + 64 * (lane >> 5) + 16 * ((lane & 3) ^ (2 * (sl >> 2)));
#define SLOT(r) ((r) % 3 == 0 ? 114688u : (r) % 3 == 1 ? 0u : 28672u)
#define KR(r) ((r) + rot >= 7 ? (r) + rot - 7 : (r) + rot)
#define P1_D1(r, k) do { if ((k) == 0) dma(SLOT(r) + 2048 * w, vx0_, sx, KR(r) * 128); if ((k) == 2) dma(SLOT(r) + 2048 * w + 1024, vx1_, sx, KR(r) * 128); \
                     if ((k) == 1) dma(SLOT(r) + 16384 + 1536 * w, vw_, sw, KR(r) * (64 * 2688)); if ((k) == 3) dma_lo32(SLOT(r) + 16384 + 1536 * w + 1024, vw_, sw, KR(r) * (64 * 2688) + 128); } while (0)
#define P1_D(r) do { P1_D1(r, 0); P1_D1(r, 1); P1_D1(r, 2); P1_D1(r, 3); } while (0)
    u32 vx0_ = vx0, vx1_ = vx1, vw_ = vw;
    PIN(vx0_); PIN(vx1_); PIN(vw_);
    asm volatile("s_waitcnt lgkmcnt(0)" : "+s"(ka), "+s"(kw_));
    char* const ws = (char*)kw_;
    const u32x4 sx = {ka[0], ka[1] & 0xffffu, 2048 * 896, 0x00020000u}, sw = {ka[2], ka[3] & 0xffffu, 448 * 2688, 0x00020000u};
    if (__builtin_expect(p3act, 1)) { P1_D(0); }
    STAMPG(1, 17);
    SB();
    const u32 i = lane & 15, g = lane >> 4, q = (lane >> 2) & 3, p = lane & 3;
    const u32x4 ones = {0x3f803f80u, 0x3f803f80u, 0x3f803f80u, 0x3f803f80u};
    rsrc_t rws = __builtin_amdgcn_make_buffer_rsrc(ws, 0, WS_END + 65536 * 4, 0x00020000);
    PINS(rws);
    const u32 s7 = (s * 37) >> 8;
    const u32 tb = 4 * xcd + s7, nb = s - 7 * s7;
    const u32 T = 4 * (xcd >> 1) + (s >> 3), h = 8 * (xcd & 1) + (s & 7);
    const u32 tg = 4 * (xcd >> 2) + (s >> 3), fs = 8 * (xcd & 3) + (s & 7);
    const u32 wl = w & 3, hf = w >> 2;
#define PREWARM(third) do { \
        u32 a0_ = WS_FB + ((8 * T + w) * 16 + h) * 128, a1_ = WS_FD + (((4 * tg + wl) * 2 + hf) * 32 + fs) * 128, a2_ = (third); \
        asm volatile("" : "+s"(a0_), "+s"(a1_), "+s"(a2_)); \
        const u32 pwo_ = (lane < 8 ? a0_ : lane < 16 ? a1_ : a2_) + (lane & 7) * 16; \
        const u64 pwm_ = w < 3 ? 0xffffffull : 0xffffull; \
        asm volatile("s_mov_b64 exec, %2\n\tbuffer_store_dwordx4 %0, %1, %3, 0 offen sc1\n\ts_mov_b64 exec, -1\n\ts_nop 0" :: "v"(ones), "v"(pwo_), "s"(pwm_), "s"(rws) : "memory"); \
    } while (0)
    u64 tag;
    u32x2 xr0 = {0, 0}, xr1 = {0, 0};
#define XLD() do { const rsrc_t rx = __builtin_amdgcn_make_buffer_rsrc((void*)gx, 0, 2048 * 896, 0x00020000); \
        const u32 xo = (64 * tb + 16 * wl + i) * 896 + (64 * nb + 32 * hf + 4 * g) * 2; \
        xr0 = __builtin_amdgcn_raw_buffer_load_b64(rx, xo, 0, 0); xr1 = __builtin_amdgcn_raw_buffer_load_b64(rx, xo + 32, 0, 0); } while (0)
    constexpr float CQ = 0.18898223650461362f * 1.4426950408889634f;

    const u32 gl = lane < 56 ? lane : 55;
    const char* gup = ((gl % 14) < 7 ? gwg + (gl % 14) * 16 : gwu + (gl % 14 - 7) * 16) + (4 * w + gl / 14) * 3584 + fs * 112;
#define GULO(k) do { const u32 a_ = ((k) & 4) + (((k) + (s >> 3)) & 3); dma_g56((32 * a_ + 4 * w) * 224, gup + a_ * (32 * 3584)); } while (0)
    const u32 r6 = (3 * (s >> 3)) >> 1;
#define GUHI1(k) do { const u32 a_ = (k) + r6 >= 6 ? (k) + r6 - 6 : (k) + r6; dma_g56(L_GUHI + (32 * a_ + 4 * w) * 224, gup + (8 + a_) * (32 * 3584)); } while (0)
#define GUTOUCH() do { const u32 l_ = lane < 28 ? lane : 27, c_ = l_ % 14, w_ = w < 7 ? w : 6; \
        const char* p_ = (c_ < 7 ? gwg + c_ * 128 : gwu + (c_ - 7) * 128) + (14 * s + 2 * w_ + l_ / 14) * 3584 + (xcd & 3) * 896; \
        asm volatile("s_mov_b32 m0, %0\n\ts_nop 0\n\tglobal_load_lds_dword %1, off" :: "n"(L_DUMMY), "v"(p_) : "memory"); } while (0)

    if (__builtin_expect(p3act, 1)) {
        asm volatile("s_barrier" ::: "memory");
        P1_D(1);
        const u64 ep = *(const CAS u64*)(ws + WS_EPOCH);
        const u32 tl0 = 2 * wl + hf;
        const u32 xb = 1024 * (i >> 3) + 128 * (i & 7) + 16 * (g ^ ((i >> 1) & 7));
        const u32 xa00 = 2048 * tl0 + xb, xa01 = xa00 ^ 64, xa10 = xa00 ^ 2048, xa11 = xa00 ^ (2048 + 64);
        const u32 wb = 16384 + 3072 * (g >> 1) + 64 * (4 * (g & 1) + q) + 8 * p;
        u32 ct[3], wa[3];
#pragma unroll
        for (int j = 0; j < 3; j++) { const u32 jj = j + wl; ct[j] = 3 * hf + (jj >= 3 ? jj - 3 : jj); wa[j] = wb + 32 * ((ct[j] & 1) ^ (g & 1)) + 512 * (ct[j] >> 1); }
        u32 ya00 = xa00 + 114688, ya01 = xa01 + 114688, ya10 = xa10 + 114688, ya11 = xa11 + 114688, yw[3] = {wa[0] + 114688, wa[1] + 114688, wa[2] + 114688};
        PIN(ya00); PIN(ya01); PIN(ya10); PIN(ya11); PIN(yw[0]); PIN(yw[1]); PIN(yw[2]);
        const u32 vwo = (lane >> 3) * 896 + nb * 128 + 16 * ((lane & 7) ^ (2 * ((lane >> 4) & 3)));
        const u32x4 so = mksrd(gwo, 448 * 896);
#define WO_D(k) do { const u32 kk = (k) + 2 * s7 >= 7 ? (k) + 2 * s7 - 7 : (k) + 2 * s7; dma(L_WO + (8 * kk + w) * 1024, vwo, so, (8 * kk + w) * 8 * 896); } while (0)
        f32x4 acc[2][3], cs = {0, 0, 0, 0}, sm = {0, 0, 0, 0}, gr = {0, 0, 0, 0};
#pragma unroll
        for (int j = 0; j < 3; j++) { acc[0][j] = (f32x4){0, 0, 0, 0}; acc[1][j] = (f32x4){0, 0, 0, 0}; }
        u32x4 xf[2][2][2], wf[2][2][3];
#define XA(r, A, Y) ((r) % 3 == 0 ? Y : SLOT(r) + A)
#define WA(r, j) ((r) % 3 == 0 ? yw[j] : SLOT(r) + wa[j])
#define P1_R1(r, k) do { \
            if ((k) == 0) xf[(r) & 1][0][0] = lds128(XA(r, xa00, ya00)); if ((k) == 1) wf[(r) & 1][0][0] = trfrag(WA(r, 0), WA(r, 0) + 1536); \
            if ((k) == 2) xf[(r) & 1][0][1] = lds128(XA(r, xa10, ya10)); if ((k) == 3) wf[(r) & 1][0][1] = trfrag(WA(r, 1), WA(r, 1) + 1536); \
            if ((k) == 4) wf[(r) & 1][0][2] = trfrag(WA(r, 2), WA(r, 2) + 1536); \
            if ((k) == 5) xf[(r) & 1][1][0] = lds128(XA(r, xa01, ya01)); if ((k) == 6) wf[(r) & 1][1][0] = trfrag(WA(r, 0) + 6144, WA(r, 0) + 7680); \
            if ((k) == 7) xf[(r) & 1][1][1] = lds128(XA(r, xa11, ya11)); if ((k) == 8) wf[(r) & 1][1][1] = trfrag(WA(r, 1) + 6144, WA(r, 1) + 7680); \
            if ((k) == 9) wf[(r) & 1][1][2] = trfrag(WA(r, 2) + 6144, WA(r, 2) + 7680); } while (0)
        {
            u32 fa_ = WS_FA + (T1 * 14 + blk) * 128, fc_ = WS_FC + ((tb * 7 + nb) * 2 + (w & 1)) * 128;
            asm volatile("" : "+s"(fa_), "+s"(fc_));
            PREWARM(w == 0 ? fa_ : fc_);
        }
        u32 ra[2][3], ca[3], qa[3], swa, cwa, xo_ = (64 * tb + 16 * wl + i) * 896 + (64 * nb + 32 * hf + 4 * g) * 2;
        u64 mk1 = 0xAAAAAAAAAAAAAAAAull, mk2 = 0xCCCCCCCCCCCCCCCCull;
        {
            const u32 qo = WS_QKV + (((8 * T1 + (tl0 ^ (g & 1))) * 168 + 12 * blk + (g >> 1)) * 16 + i) * 16;
            const u32 rb_ = L_SCR + 512 + 256 * tl0 + 16 * i;
#pragma unroll
            for (int j = 0; j < 3; j++) {
                ra[0][j] = rb_ + (6 * blk + ct[j] < 28 ? 8 : 0); ra[1][j] = ra[0][j] ^ 256; ca[j] = L_SCR + 64 * ct[j] + 16 * g; qa[j] = qo + ct[j] * 512;
                PIN(ra[0][j]); PIN(ra[1][j]); PIN(ca[j]); PIN(qa[j]);
            }
            swa = g == (i >> 2) ? rb_ : L_DUMMY + lane * 16; cwa = i == 0 ? ca[0] : L_DUMMY + lane * 16;
            PIN(swa); PIN(cwa); PIN(xo_); PINS(mk1); PINS(mk2);
        }
        WAIT_BAR(5);
        STAMPG(1, 18);
        SFOR(k, 0, 10) P1_R1(0, k); SEND
        SB();
        P1_D(2);
        tag = mix(ep);
        PINS(tag);
#define P1_M(B, cl, m) do { \
            if ((m) == 0) acc[0][0] = mfma(wf[B][cl][0], xf[B][cl][0], acc[0][0]); if ((m) == 1) sm = mfma(ones, xf[B][cl][0], sm); \
            if ((m) == 2) gr = mfma(xf[B][cl][0], xf[B][cl][0], gr); if ((m) == 3) cs = mfma(wf[B][cl][0], ones, cs); \
            if ((m) == 4) acc[1][0] = mfma(wf[B][cl][0], xf[B][cl][1], acc[1][0]); \
            if ((m) == 5) acc[0][1] = mfma(wf[B][cl][1], xf[B][cl][0], acc[0][1]); if ((m) == 6) acc[1][1] = mfma(wf[B][cl][1], xf[B][cl][1], acc[1][1]); \
            if ((m) == 7) acc[0][2] = mfma(wf[B][cl][2], xf[B][cl][0], acc[0][2]); if ((m) == 8) acc[1][2] = mfma(wf[B][cl][2], xf[B][cl][1], acc[1][2]); } while (0)
        SFOR(r, 0, 5)
            constexpr int NY[6] = {5, 4, 4, 4, 4, (GPOS == 0 ? 7 : 3) + TGU};
            WAIT_BAR(NY[r]);
            STAMPG(1, 19 + r);
            SFOR(m, 0, 18)
                P1_M(r & 1, m / 9, m % 9);
                if constexpr (XE == 4 + r && m == 0) XLD();
                constexpr int RD[18] = {0, -1, 1, 2, 3, -1, 4, 5, 6, 7, -1, 8, 9, -1, -1, -1, -1, -1};
                constexpr int DP[18] = {-1, 0, -1, -1, -1, 1, -1, -1, -1, -1, 2, -1, -1, -1, 3, -1, -1, -1};
                if constexpr (RD[m] >= 0) P1_R1(r + 1, RD[m]);
                if constexpr (r < 4 && DP[m] >= 0) P1_D1(r + 3, DP[m]);
                if constexpr (GPOS == 0) {
                    if constexpr (r == 4 && m < 14 && (m & 1) == 0) WO_D(m / 2);
                    if constexpr (r == 5 && m < 16 && (m & 3) == 0) GULO(m / 4);
                } else {
                    if constexpr (TGU && r == 4 && m == 0) GUTOUCH();
                    if constexpr (r == 4 && (m % 6) == 1) WO_D(m / 6);
                    if constexpr (r == 5 && (m % 9) == 1) WO_D(3 + m / 9);
                }
                SB();
            SEND
        SEND
        WAIT_BAR(3);
        STAMPG(1, 24);
        f32x2 rm[2][3]; f32x4 cc[3], mc[2][3];
#define P1_E(j) do { const f32x4 v0 = acc[0][j] * rm[0][j][0] + mc[0][j], v1 = acc[1][j] * rm[1][j][0] + mc[1][j]; \
            auto a = __builtin_amdgcn_permlane16_swap(pk2(v0[0], v0[1]), pk2(v1[0], v1[1]), false, false); \
            auto bb = __builtin_amdgcn_permlane16_swap(pk2(v0[2], v0[3]), pk2(v1[2], v1[3]), false, false); \
            u32x4 o = {a[0], bb[0], a[1], bb[1]}; st_sc1(rws, o, qa[j], 0); } while (0)
        SFOR(n, 0, 36)
            constexpr int ORD[36] = {1, 2, 3, 10, 11, 12, 0, 4, 9, 13, 19, 20, 21, 28, 29, 30, 5, 6, 14, 15, 7, 8, 16, 17,
                                     18, 22, 27, 31, 23, 24, 32, 33, 25, 26, 34, 35};
            constexpr int RD[10] = {0, 5, 1, 6, 2, 7, 3, 8, 4, 9};
            P1_M(1 - ORD[n] / 18, (ORD[n] % 18) / 9, ORD[n] % 9);
            if constexpr (n < 10) P1_R1(6, RD[n]);
            if constexpr (n == TW0 || n == TW0 + 3 || n == TW0 + 6 || n == TW0 + 9) WO_D(3 + (n - TW0) / 3);
            if constexpr (n == TXL) {
                const rsrc_t rx = __builtin_amdgcn_make_buffer_rsrc((void*)gx, 0, 2048 * 896, 0x00020000);
                xr0 = __builtin_amdgcn_raw_buffer_load_b64(rx, xo_, 0, 0); xr1 = __builtin_amdgcn_raw_buffer_load_b64(rx, xo_ + 32, 0, 0);
            }
            if constexpr (n == TST) {
                const bool b1 = __builtin_amdgcn_inverse_ballot_w64(mk1), b2 = __builtin_amdgcn_inverse_ballot_w64(mk2);
                float g0 = gr[0], g2 = gr[2];
                pinv(g0); pinv(g2);
                const float d01 = b1 ? gr[1] : g0, d23 = b1 ? gr[3] : g2, a2 = b2 ? d23 : d01;
                const float mean = sm[0] * (1.f / 448.f);
                const float var = fmaxf(a2 * (1.f / 448.f) - mean * mean, 0.f);
                const float rs = __builtin_amdgcn_rsqf(var + 1e-5f), ms = -mean * rs;
                ldsf4w(cwa, cs);
                ldsf4w(swa, (f32x4){rs, ms, rs * CQ, ms * CQ});
            }
            if constexpr (n == TBR) {
                BAR();
                STAMPG(1, 26);
#pragma unroll
                for (int j = 0; j < 3; j++) { rm[0][j] = ldsf2(ra[0][j]); rm[1][j] = ldsf2(ra[1][j]); cc[j] = ldsf4(ca[j]); }
            }
            if constexpr (n >= TMC && n < TMC + 6) { constexpr int t = (n - TMC) & 1, j = (n - TMC) >> 1; mc[t][j] = cc[j] * rm[t][j][1]; pinv(mc[t][j]); }
            if constexpr (n == TE0) P1_E(0);
            if constexpr (n == TE0 + 4) P1_E(1);
            SB();
        SEND
        P1_E(2);
        if constexpr (TXL == 36) {
            SB();
            const rsrc_t rx = __builtin_amdgcn_make_buffer_rsrc((void*)gx, 0, 2048 * 896, 0x00020000);
            xr0 = __builtin_amdgcn_raw_buffer_load_b64(rx, xo_, 0, 0); xr1 = __builtin_amdgcn_raw_buffer_load_b64(rx, xo_ + 32, 0, 0);
            SB();
        }
        STAMP(1);
        if constexpr (GPOS == 1) { SB(); SFOR(k, 0, 8) GULO(k); SEND }
        publish<TXL == 36 ? 2 : 0, 1>(rws, WS_FA + (T1 * 14 + blk) * 128, w == 0 ? 1 : 0, tag);
        SFOR(k, 0, NG1) GULO(k); SEND
        if constexpr (XE == 2) { XLD(); SB(); }
    } else {
        const u32 fsh = (s - 28) * 8 + xcd;
        const u32x4 sd = mksrd(gwd, 1792 * 896);
#pragma unroll
        for (int k = 0; k < 6; k++) dma((8 * k + w) * 1024, lane * 16, sd, fsh * 50176 + (8 * k + w) * 1024);
        dma(48 * 1024, lane * 16, sd, fsh * 50176 + 48 * 1024);
        PREWARM(WS_FW + fsh * 128);
        if constexpr (TGU) GUTOUCH();
        const u64 ep = *(const CAS u64*)(ws + WS_EPOCH);
        WAIT_BAR(1 + TGU);
        const u32 a4 = fsh >> 2, pp = fsh & 3;
        const u32 ra = (4 * g + q) * 896 + 8 * p;
        const u32 rb = (g < 2 ? 32 + 4 * g + q : 48 + q) * 896 + 8 * p, rb2 = rb + (g < 2 ? 8 : 4) * 896;
        const u32 wo2 = g < 2 ? (4 + (pp >> 1)) * 4096 + (pp & 1) * 512 + lane * 16 : 6 * 4096 + pp * 256 + i * 16;
#pragma unroll
        for (int k = 0; k < 4; k++) {
            const u32 nt = w + 8 * k;
            if (nt < 28) {
                const u32 dst = WS_WT + (((nt >> 2) * 8 + a4) * 28 + (nt & 3)) * 1024;
                st_sc1(rws, trfrag(ra + 32 * nt, ra + 32 * nt + 16 * 896), pp * 4096 + lane * 16, dst);
                u32x4 v = trfrag(rb + 32 * nt, rb2 + 32 * nt);
                if (g < 3) st_sc1(rws, v, wo2, dst);
            }
        }
        tag = mix(ep);
        STAMP(1);
        WAIT_BAR(0);
        if (w == 0) set_flag1(rws, WS_FW + fsh * 128, tag);
        if constexpr (GPOS != 2) { SFOR(k, 0, 8) GULO(k); SEND }
        SFOR(k, 0, NG1) GULO(k); SEND
        asm volatile(".Lfz_e1:" ::: "memory");
    }
    asm volatile("" : "+s"(tag));
    STAMP(2);
    u32x4 wd[28];
    const u32 rot50 = nb + 2 * s7, rot5 = rot50 >= 7 ? rot50 - 7 : rot50;
#define K5(k) ((k) + rot5 >= 7 ? (k) + rot5 - 7 : (k) + rot5)
    const u32 kw = (w + KWR * nb) & 7;
    const u32 wts = WS_WT + (nb * 8 + kw) * 28 * 1024;
    u32 wtv0 = lane * 16 + hf * 2048, wtv1 = lane * 16 + (hf ^ 1) * 2048;
#define LDWTA(f) wd[f] = rld((((f) & 2) ? wtv1 : wtv0) + ((f) & 1) * 1024, rws, wts + K5((f) >> 2) * 4096)
#define LDWT(f) wd[f] = ld_sc1(rws, (((f) & 2) ? wtv1 : wtv0) + ((f) & 1) * 1024, wts + K5((f) >> 2) * 4096)

    u32 woa[2][4][2];
    {
        const u32 g1 = g == 3 ? 1 : g, fe = 2 * (g & 1) + (q >> 1);
        const u32 r0 = L_WO + (224 * hf + 4 * g + q) * 128 + 8 * p, r1 = L_WO + (224 * hf + 16 + 4 * g1 + q) * 128 + 8 * p;
#pragma unroll
        for (int k = 0; k < 8; k++) {
            const u32 sw32 = 32 * ((k & 3) ^ fe) + ((k & 4) ^ (4 * (nb & 1))) * (28 * 128);
            woa[k >> 2][k & 3][0] = r0 + sw32; woa[k >> 2][k & 3][1] = r1 + sw32;
            if (WOA) { PIN(woa[k >> 2][k & 3][0]); PIN(woa[k >> 2][k & 3][1]); }
        }
    }
    u32 cgb = (4 * g + q) * 224 + 8 * p;
    u32 cmb = (4 * g + q) * 224 + (p < 2 ? 96 : 192) + 8 * p;
    u32 cb = w >= 6 ? cmb : cgb + (w < 3 ? 32 * w : 112 + 32 * (w - 3));
    const char* gua[8];
#pragma unroll
    for (int k = 0; k < 8; k++) {
        gua[k] = gup + ((k & 4) + ((k + (s >> 3)) & 3)) * (32 * 3584);
        if (GUA & 1) PIN(gua[k]);
    }
    if (GUA & 2) { PIN(wtv0); PIN(wtv1); }
    if (GUA & 4) { PIN(cgb); PIN(cmb); PIN(cb); }
#undef GULO
#define GULO(k) do { const u32 a_ = ((k) & 4) + (((k) + (s >> 3)) & 3); dma_g56((32 * a_ + 4 * w) * 224, gua[k]); } while (0)
    {
        const u32 c0 = (7 * h) >> 1;
        u32x4 qf;
        {
            const int t0 = 8 * ((int)T - 2) + 6 * (int)wl;
            const int Tp = lane < 2 ? t0 >> 3 : lane < 4 ? (t0 + 5) >> 3 : (int)T;
            const u32 ch = (lane < 4 ? 56 + 56 * hf : 0) + c0 + 3 * (lane & 1);
            const u64 am = (t0 >= 0 ? 3 : 0) | (t0 + 5 >= 0 ? 12 : 0) | 48;
            u32 vo = (16 * g + (hf ? i ^ (8 * (g & 1)) : i)) * 16;
            u32 src = WS_QKV + (t0 * 168 + 56 + 56 * hf + c0) * 256, dst = L_K + 6144 * w;
            u32 qsrc = WS_QKV + ((8 * T + w) * 168 + c0) * 256;
            PIN(vo); PINS(src); PINS(dst); PINS(qsrc);
            wait_flags(rws, WS_FA + (Tp * 14 + ((ch * 171) >> 11)) * 128, am, tag);
            STAMP(3);
            if (__builtin_expect(t0 >= 0, 1)) {
                dma6(dst, vo, rws, src);
            } else {
#pragma unroll
                for (int n = 0; n < 6; n++) lds128w(dst + 1024 * n + lane * 16, (u32x4){0, 0, 0, 0});
                asm volatile("s_waitcnt lgkmcnt(0)" ::: "memory");
#pragma unroll 1
                for (int n = -t0; n < 6; n++) dma_r(dst + 1024 * n, vo, rws, src + n * (168 * 256));
            }
            qf = ld_sc1(rws, lane * 16, qsrc);
        }
        f32x4 m0, m16;
#pragma unroll
        for (int r = 0; r < 4; r++) { m0[r] = 4 * g + r > i ? 0.f : -1e30f; m16[r] = 4 * g + r > i ? -1e30f : 0.f; }
        const bool fg = (h & 1) ? (g == 0) : (g == 3);
        u32 zlo = (fg && (h & 1)) ? 0u : ~0u, zhi = (fg && !(h & 1)) ? 0u : ~0u;
        u32 kb = L_K + w * 1024 + lane * 16;
        const u32 bo = 8 * (h & 1) + 8 * p;
        u32 vb = L_V + w * 1024 + (bo >> 4) * 256 + (((4 * g + q) ^ (8 * ((bo >> 4) & 1))) * 16) + (bo & 8);
        u32 oo = WS_O + lane * 16 + ((8 * T + w) * 16 + h) * 1024;
        u32 bm0 = (4 * g > i ? 0xffffu : 0u) | (4 * g + 1 > i ? 0xffff0000u : 0u), bm1 = (4 * g + 2 > i ? 0xffffu : 0u) | (4 * g + 3 > i ? 0xffff0000u : 0u);
        u32x4 on = ones;
        PIN(bm0); PIN(bm1); PIN(on);
        PIN(m0); PIN(m16); PIN(zlo); PIN(zhi); PIN(kb); PIN(vb); PIN(oo);
        if (p3act) wait_flags(rws, WS_FW + (4 * kw + lane) * 128, 15, tag);
        qf[0] &= zlo; qf[1] &= zlo; qf[2] &= zhi; qf[3] &= zhi;
        PIN(qf);
        WAIT_BAR(0);
        STAMP(4);
        u32x4 kf[17];
#pragma unroll
        for (int n = 0; n < 8; n++) kf[n] = lds128(kb + n * 1024);
        SB();
        f32x4 sc[17];
        u32x2 vf[17][2];
        SFOR(n, 0, 17)
            sc[n] = mfma(kf[n], qf, n == 0 ? m0 : n == 16 ? m16 : (f32x4){0, 0, 0, 0});
            if constexpr (n + 8 < 17) kf[n + 8] = lds128(kb + (n + 8) * 1024);
            if constexpr (n >= 8) { vf[2 * n - 16][0] = trhalf(vb + (2 * n - 16) * 1024); vf[2 * n - 16][1] = trhalf(vb + (2 * n - 16) * 1024 + 512); }
            if constexpr (n >= 8 && n < 16) { vf[2 * n - 15][0] = trhalf(vb + (2 * n - 15) * 1024); vf[2 * n - 15][1] = trhalf(vb + (2 * n - 15) * 1024 + 512); }
            if constexpr (TWT && n == TWT - 1) {
                const u32 tv_ = (lane < 56 && p3act) ? WS_WT + ((s >> 2) * 8 + w) * 28672 + ((s & 3) * 56 + lane) * 128 : 0xF0000000u;
                asm volatile("s_mov_b32 m0, %0\n\ts_nop 0\n\tbuffer_load_dword %1, %2, 0 offen sc1 lds" :: "s"((32 * (4 + ((7 + (s >> 3)) & 3)) + 4 * w) * 224), "v"(tv_), "s"(mksrd(ws, WS_END)) : "memory");
            }
            if constexpr (!GHI && (n & 3) == 2 && NG1 + n / 4 < 8) GULO(NG1 + n / 4);
            if constexpr (XE == 3 && n == 0) XLD();
            if constexpr (GHI && (n % 3) == 1 && NG1 + n / 3 < 8) GULO(NG1 + n / 3);
            if constexpr (GPOS != 2 && (n & 3) == 2 && n / 4 < NWT1) LDWT(n / 4);
            SB();
        SEND
        STAMPG(4, 32);
        if constexpr (GHI) { BAR(); SFOR(k, NG1 + 6, 8) GULO(k); SEND }
        const int nneg = 16 - (int)(8 * T + w);
        if (nneg > 0) {
#pragma unroll
            for (int n = 0; n < 16; n++) if (n < nneg) sc[n] = (f32x4){-1e30f, -1e30f, -1e30f, -1e30f};
        }
#pragma unroll
        for (int r = 0; r < 4; r++) sc[0][r] = mx3(sc[0][r], sc[16][r], -3e38f);
        float mx[4];
#pragma unroll
        for (int n = 0; n < 4; n++) mx[n] = mx3(sc[n + 1][0], sc[n + 1][1], sc[n + 1][2]);
        mx[0] = mx3(mx[0], sc[1][3], sc[2][3]); mx[1] = mx3(mx[1], sc[3][3], sc[4][3]);
#pragma unroll
        for (int n = 5; n < 17; n++) { mx[n & 3] = mx3(mx[n & 3], sc[n & 15][0], sc[n & 15][1]); mx[(n + 2) & 3] = mx3(mx[(n + 2) & 3], sc[n & 15][2], sc[n & 15][3]); }
        const float m = xmax(mx3(mx3(mx[0], mx[1], mx[2]), mx[3], -3e38f));
        u32 pl0 = 0, pl1 = 0;
        f32x4 o0 = {0, 0, 0, 0}, o1 = {0, 0, 0, 0}, ol = {0, 0, 0, 0};
        STAMPG(4, 33);
        constexpr int JL = T16 ? 7 : 8;
        SFOR(j, 0, JL + 1)
            u32x4 pb = {pl0, pl1, 0u, 0u};
            if constexpr (j < 8) {
                const f32x4 a = sc[2 * j] - m;
                pb[0] = pk2(__builtin_amdgcn_exp2f(a[0]), __builtin_amdgcn_exp2f(a[1])); pb[1] = pk2(__builtin_amdgcn_exp2f(a[2]), __builtin_amdgcn_exp2f(a[3]));
            }
            if constexpr (j == 0) { pl0 = pb[0] & ~bm0; pl1 = pb[1] & ~bm1; pb[0] &= bm0; pb[1] &= bm1; }
            if constexpr (j < 8) {
                const f32x4 c = sc[2 * j + 1] - m;
                pb[2] = pk2(__builtin_amdgcn_exp2f(c[0]), __builtin_amdgcn_exp2f(c[1])); pb[3] = pk2(__builtin_amdgcn_exp2f(c[2]), __builtin_amdgcn_exp2f(c[3]));
            }
            constexpr int j1 = j < 8 ? 2 * j + 1 : 15;
            if constexpr (OLF && j == JL) { ol = mfma(on, pb, ol); SB(); }
            o0 = mfma((u32x4){vf[2 * j][0][0], vf[2 * j][0][1], vf[j1][0][0], vf[j1][0][1]}, pb, o0);
            o1 = mfma((u32x4){vf[2 * j][1][0], vf[2 * j][1][1], vf[j1][1][0], vf[j1][1][1]}, pb, o1);
            if constexpr (!(OLF && j == JL)) ol = mfma(on, pb, ol);
            if constexpr (T16 && j == T16 - 1) {
                const u32x2 pl = {pl0, pl1};
                o0 = mfma16(vf[16][0], pl, o0); o1 = mfma16(vf[16][1], pl, o1); ol = mfma16((u32x2){on[0], on[1]}, pl, ol);
            }
            if constexpr (GPOS != 2 && 4 + j < NWT1) LDWT(4 + j);
            if constexpr (!GHI && NG1 + 4 + j < 8) GULO(NG1 + 4 + j);
            if constexpr (GHI && j < 6) GUHI1(j);
            if constexpr (GPOS == 2 && j < NWT1) LDWT(j);
            SB();
        SEND
        const float rl = __builtin_amdgcn_rcpf(ol[0]);
        o0 *= rl; o1 *= rl;
        u32x4 of = {pk2(o0[0], o0[1]), pk2(o0[2], o0[3]), g == 3 ? 0u : pk2(o1[0], o1[1]), g == 3 ? 0u : pk2(o1[2], o1[3])};
        st_sc1(rws, of, oo, 0);
        STAMP(5);
    }

    u32x4 hres = {0, 0, 0, 0};
    f32x4 cs4 = {0, 0, 0, 0};
#define GUHI() do { if constexpr (!GHI) { SFOR(k, 0, 6) GUHI1(k); SEND } } while (0)
#define CSLO() do { u32x4 f_[8]; _Pragma("unroll") for (int c = 0; c < 8; c++) f_[c] = trfrag(cb + c * 7168, cb + c * 7168 + 3584); SB(); \
                    _Pragma("unroll") for (int c = 0; c < 8; c++) cs4 = mfma(f_[c], ones, cs4); } while (0)
    if (__builtin_expect(p3act, 1)) {
        u32x4 wf[8][4];
        const u32 hx = 4 * (nb & 1);
#pragma unroll
        for (int hl = 0; hl < 8; hl++)
#pragma unroll
            for (int t = 0; t < 4; t++)
                wf[hl][t] = trfrag(woa[hl >> 2][t ^ (2 * (hl & 1))][0] + (hl & 3) * 28 * 128, woa[hl >> 2][t ^ (2 * (hl & 1))][1] + (hl & 3) * 28 * 128);
        publish<0, 0>(rws, WS_FB + ((8 * T + w) * 16 + h) * 128, 1, tag);
        BAR();
        STAMP(6);
        SFOR(k, 0, GH1) GUHI1(k); SEND
        if constexpr (XE == 0) XLD();
        if constexpr (!W2L) { SFOR(f, NWT1, NWT1 + NWT2) LDWT(f); SEND }
        SB();
        CSLO();
        u32 ho = WS_H + lane * 16 + ((4 * tb + wl) * 14 + 2 * nb + hf) * 1024, so_ = WS_ST + lane * 8 + ((4 * tb + wl) * 14 + 2 * nb + hf) * 128;
        u32 ex0 = L_WO + (w ^ 4) * 2048 + lane * 16, ex1 = L_WO + w * 2048 + lane * 16;
        PIN(ho); PIN(so_); PIN(ex0); PIN(ex1); PIN(cs4);
        wait_flags<(APOLL >> 1) & 1>(rws, WS_FB + ((4 * tb + wl) * 16 + 8 * hf + lane) * 128, 255, tag);
        STAMP(7);
        u32x4 of[8];
        const u32 ob = WS_O + ((4 * tb + wl) * 16 + 8 * hf) * 1024;
        constexpr int NPO = 6 - GH1 + (W2L ? NWT2 : 0);
        u32 ov[4];
#pragma unroll
        for (int k = 0; k < 4; k++) ov[k] = lane * 16 + k * 1024;
        SFOR(hl, 0, 8) of[hl] = rld(ov[hl & 3], rws, ob + ((hl & 4) ^ hx) * 1024); SEND
        SFOR(k, GH1, 6) GUHI1(k); SEND
        if constexpr (W2L) { SFOR(f, NWT1, NWT1 + NWT2) LDWT(f); SEND }
        SB();
        f32x4 acc[4];
        SFOR(hl, 0, 8)
            aw<7 - hl + NPO>(of[hl]);
#pragma unroll
            for (int t = 0; t < 4; t++) acc[t] = mfma(wf[hl][t], of[hl], hl == 0 ? (f32x4){0, 0, 0, 0} : acc[t]);
            SB();
        SEND
        ldsf4w(ex0, hf ? acc[0] : acc[2]);
        ldsf4w(ex0 + 1024, hf ? acc[1] : acc[3]);
        f32x4 h0 = hf ? acc[2] : acc[0], h1 = hf ? acc[3] : acc[1];
        h0 += (f32x4){bflo(xr0[0]), bfhi(xr0[0]), bflo(xr0[1]), bfhi(xr0[1])};
        h1 += (f32x4){bflo(xr1[0]), bfhi(xr1[0]), bflo(xr1[1]), bfhi(xr1[1])};
        PIN(h0); PIN(h1);
        BAR();
        h0 += ldsf4(ex1); h1 += ldsf4(ex1 + 1024);
        hres[0] = pk2(h0[0], h0[1]); hres[1] = pk2(h0[2], h0[3]); hres[2] = pk2(h1[0], h1[1]); hres[3] = pk2(h1[2], h1[3]);
        st_sc1(rws, hres, ho, 0);
        {
            float s1 = dot2(hres[0], 0x3f803f80u, 0.f), s2 = dot2(hres[0], hres[0], 0.f), s3 = dot2(hres[1], 0x3f803f80u, 0.f), s4 = dot2(hres[1], hres[1], 0.f);
            s1 = dot2(hres[2], 0x3f803f80u, s1); s2 = dot2(hres[2], hres[2], s2); s3 = dot2(hres[3], 0x3f803f80u, s3); s4 = dot2(hres[3], hres[3], s4);
            s1 = xsum(s1 + s3); s2 = xsum(s2 + s4);
            u32x4 v = {asu(s1), asu(s2), (u32)__builtin_amdgcn_mov_dpp((int)asu(s1), 0xB1, 0xf, 0xf, false), (u32)__builtin_amdgcn_mov_dpp((int)asu(s2), 0xB1, 0xf, 0xf, false)};
            if ((lane & 49) == 0) st_sc1(rws, v, so_, 0);
        }
        STAMP(8);
    } else {
        publish<0, 0>(rws, WS_FB + ((8 * T + w) * 16 + h) * 128, 1, tag);
        BAR();
        GUHI();
        CSLO();
        if (b == 255 && w == 0) {
#pragma unroll
            for (int k = 0; k < 4; k++) wait_flags(rws, WS_FB + ((4 * k + g) * 128 + i) * 128, ~0ull, tag);
            set_flag1(rws, WS_EPOCH, tag);
        }
        WAIT_BAR(0);
        asm volatile(".Lfz_e2:" ::: "memory");
    }

    u32 aof[7], av[2][2], fd1, fd0 = WS_FD + (tb * 2 * 32 + 4 * kw + lane) * 128;
    {
#define CSHI() do { u32x4 f_[6]; const u32 cb2 = cb + L_GUHI; _Pragma("unroll") for (int c = 0; c < 6; c++) f_[c] = trfrag(cb2 + c * 7168, cb2 + c * 7168 + 3584); SB(); \
        _Pragma("unroll") for (int c = 0; c < 6; c++) cs4 = mfma(f_[c], ones, cs4); if (w < 7 && i == 0) ldsf4w(L_SCR + 64 * w + 16 * g, cs4); } while (0)
        if constexpr (GH1 == 6) CSHI();
        const u32 tb4 = 4 * tg + wl, tt0 = 4 * tb4 + 2 * hf;
        const u32 rc = ((fs & 7) * 7) >> 2;
#define C4(c) ((c) + rc >= 14 ? (c) + rc - 14 : (c) + rc)
#define LO4(c) (C4(c) * 7168 + (C4(c) >= 8 ? 57344 : 0))
        u32x4 wf[PD + 1];
        u32 ga[14], gm[14], hof[14], lo4[14];
        const u32 hs = WS_H + tt0 * 14 * 1024;
        lo4[0] = rc * 7168 + (rc >= 8 ? 57344 : 0);
        ga[0] = cgb + lo4[0]; gm[0] = cmb + lo4[0];
#define GUA(j) ((j) % 7 == 6 ? gm[(j) / 7] : ga[(j) / 7] + ((j) % 7 < 3 ? 32 * ((j) % 7) : 112 + 32 * ((j) % 7 - 3)))
        if constexpr (GH1 == 6) { SFOR(j, 0, PD) wf[j] = trfrag(GUA(j), GUA(j) + 3584); SEND }
        u32 fco = WS_FC + ((tb4 * 7 + lane) * 2 + hf) * 128;
        u32 hv0 = lane * 16, hv1 = lane * 16 + 14 * 1024;
        u32 svo = g * 128 + i * 8, svo3 = (svo & 255) + 1536;
        PIN(fco); PIN(hv1); PIN(svo); PIN(svo3);
        lds32w(L_DONE + 4 * w, 0);
        publish<0, 1>(rws, WS_FC + ((tb * 7 + nb) * 2 + (lane & 1)) * 128, p3act && w == 0 ? 3 : 0, tag);
        if (p3act) {
#pragma unroll
            for (int f = NWT1 + NWT2; f < NWT1 + NWT2 + NWT3; f++) LDWT(f);
        }
        SB();
        u32 sto[2] = {WS_ST + tt0 * 14 * 128, WS_ST + (tt0 + 1) * 14 * 128};
        PINS(sto[0]); PINS(sto[1]);
        hof[0] = hs + rc * 1024; PINS(hof[0]);
        SFOR(c, 1, 14)
            const u32 a_ = hof[c - 1] + 1024, b_ = lo4[c - 1] + 7168;
            hof[c] = a_ == hs + 14 * 1024 ? hs : a_;
            lo4[c] = a_ == hs + 14 * 1024 ? 0 : b_ == 57344 ? 114688 : b_;
            pins(hof[c]); pins(lo4[c]);
        SEND
        if constexpr (GH1 < 6) { SB(); CSHI(); SFOR(j, 0, PD) wf[j] = trfrag(GUA(j), GUA(j) + 3584); SEND BAR(); }
        if (PB && hf) __builtin_amdgcn_s_setprio(3);
        wait_flags<(APOLL >> 2) & 1>(rws, fco, 127, tag);
        STAMP(9);
        u32x4 hw[HW][2];
#pragma unroll
        for (int c = 0; c < HW0; c++) { hw[c][0] = ld_sc1(rws, hv0, hof[c]); hw[c][1] = ld_sc1(rws, hv1, hof[c]); SB(); if (PB && c == PB - 1) { __builtin_amdgcn_s_setprio(0); if (hf == 0) __builtin_amdgcn_s_setprio(1); SB(); } }
        u32x2 sv[2][4];
#define SVLD() do { _Pragma("unroll") for (int t = 0; t < 2; t++) _Pragma("unroll") for (int j = 0; j < 4; j++) \
                    sv[t][j] = ld2_sc1(rws, j == 3 ? svo3 : svo + 512 * j, sto[t]); } while (0)
        if constexpr (SVC < 0) SVLD();
        SB();
        if (hf == 0) __builtin_amdgcn_s_setprio(2);
        else if (PRI) __builtin_amdgcn_s_setprio(1);
        f32x4 acc[2][7];
#pragma unroll
        for (int t = 0; t < 2; t++)
#pragma unroll
            for (int n = 0; n < 7; n++) acc[t][n] = (f32x4){0, 0, 0, 0};
        float mean[2], kx[2], iv[2];
        f32x4 cvn;
        u32 cva = L_SCR + 16 * g;
        PIN(cva); PIN(fd0);
        SB();
        SFOR(j, 0, 98)
            constexpr int c = j / 7, n = j % 7;
            if constexpr (n == 0 && c % 3 == 1) STAMPG(8, 34 + c / 3);
            if constexpr (n == 0 && c + 1 < 14) { ga[c + 1] = cgb + lo4[c + 1]; gm[c + 1] = cmb + lo4[c + 1]; }
            if constexpr (j + PD < 98) wf[(j + PD) % (PD + 1)] = trfrag(GUA(j + PD), GUA(j + PD) + 3584);
            if constexpr (MC) {
                constexpr int k = (j - 7 * MC) / 8, ph = (j - 7 * MC) % 8;
                if constexpr (j >= 7 * MC && k < 7) {
                    if constexpr (ph == 0) cvn = ldsf4(cva + 64 * k);
                    if constexpr (ph == 3 || ph == 4) { acc[ph - 3][k] += cvn * mean[ph - 3]; pinv(acc[ph - 3][k]); }
                }
            }
            SB();
            acc[0][n] = mfma(wf[j % (PD + 1)], hw[c % HW][0], acc[0][n]);
            acc[1][n] = mfma(wf[j % (PD + 1)], hw[c % HW][1], acc[1][n]);
            if constexpr (PINA) { pinv(acc[0][n]); pinv(acc[1][n]); }
            if constexpr (j >= HWJ && j < HWJ + HW - HW0) { constexpr int c2 = HW0 + j - HWJ; hw[c2][0] = ld_sc1(rws, hv0, hof[c2]); hw[c2][1] = ld_sc1(rws, hv1, hof[c2]); }
            if constexpr (n == 1 && c == SVC) SVLD();
            if constexpr (n == 6 && c + HW < 14) { hw[c % HW][0] = ld_sc1(rws, hv0, hof[c + HW]); hw[c % HW][1] = ld_sc1(rws, hv1, hof[c + HW]); }
            {
                constexpr int F0 = NWT1 + NWT2 + NWT3, NR = 28 - NWT5 - F0;
                if constexpr (n == 3 && c >= 14 - NR && NR <= 14) LDWT(F0 + c - (14 - NR));
                if constexpr (NR > 14 && n == 3) LDWT(F0 + c);
                if constexpr (NR > 14 && n == 1 && c < NR - 14) LDWT(F0 + 14 + c);
            }
            if constexpr (j == 7 * (SVC + 1 + HW) + 1 || j == 7 * (SVC + 1 + HW) + 3) {
                constexpr int t = j == 7 * (SVC + 1 + HW) + 3;
                float a1 = asf(sv[t][0][0]) + asf(sv[t][1][0]) + asf(sv[t][2][0]) + (g < 2 ? asf(sv[t][3][0]) : 0.f);
                float a2 = asf(sv[t][0][1]) + asf(sv[t][1][1]) + asf(sv[t][2][1]) + (g < 2 ? asf(sv[t][3][1]) : 0.f);
                a1 = xsum(a1); a2 = xsum(a2);
                mean[t] = a1 * (-1.f / 448.f);
                iv[t] = fmaxf(a2 * (1.f / 448.f) - mean[t] * mean[t], 0.f) + 1e-5f;
                kx[t] = __builtin_amdgcn_rsqf(iv[t]) * -1.4426950408889634f;
                PIN(mean[t]); PIN(iv[t]); PIN(kx[t]);
            }
            SB();
        SEND
        lds32w(L_DONE + 4 * w, 1);
        STAMP(10);
        if (PRI == 1 && hf == 0) __builtin_amdgcn_s_setprio(0);
        if (PRI == 2) __builtin_amdgcn_s_setprio(0);
        f32x4 cv[7];
#pragma unroll
        for (int n = 0; n < 7; n++) cv[n] = MC ? (f32x4){0, 0, 0, 0} : ldsf4(L_SCR + 64 * n + 16 * g);
        u32 pa[2][3][2]; f32x4 mm[2];
        const u32 a4 = fs >> 2, pp = fs & 3;
#pragma unroll
        for (int t = 0; t < 2; t++) {
#pragma unroll
            for (int n = 0; n < 3; n++) {
                const f32x4 ga_ = MC ? acc[t][n] : cv[n] * mean[t] + acc[t][n];
                const f32x4 ua = MC ? acc[t][3 + n] : cv[3 + n] * mean[t] + acc[t][3 + n];
                const f32x4 ex = ga_ * kx[t];
                f32x4 e = {__builtin_amdgcn_exp2f(ex[0]), __builtin_amdgcn_exp2f(ex[1]), __builtin_amdgcn_exp2f(ex[2]), __builtin_amdgcn_exp2f(ex[3])};
                e = e * iv[t] + iv[t];
                const f32x4 rc_ = {__builtin_amdgcn_rcpf(e[0]), __builtin_amdgcn_rcpf(e[1]), __builtin_amdgcn_rcpf(e[2]), __builtin_amdgcn_rcpf(e[3])};
                const f32x4 av = ga_ * rc_ * ua;
                pa[t][n][0] = pk2(av[0], av[1]); pa[t][n][1] = pk2(av[2], av[3]);
            }
            mm[t] = MC ? acc[t][6] : cv[6] * mean[t] + acc[t][6];
            u32x4 v = {pa[t][0][0], pa[t][0][1], pa[t][1][0], pa[t][1][1]};
            st_sc1(rws, v, WS_ACT + lane * 16, ((tt0 + t) * 56 + 7 * a4 + pp) * 1024);
        }
        {
            auto x0 = __builtin_amdgcn_permlane32_swap(pa[0][2][0], pa[1][2][0], false, false);
            auto x1 = __builtin_amdgcn_permlane32_swap(pa[0][2][1], pa[1][2][1], false, false);
            u32x4 v = {x0[0], x1[0], x0[1], x1[1]};
            st_sc1(rws, v, WS_ACT + (g >> 1) * (56 * 1024) + ((g & 1) * 16 + i) * 16, (tt0 * 56 + 7 * a4 + 4 + (pp >> 1)) * 1024 + (pp & 1) * 512);
        }
        {
            const float kq = g < 2 ? kx[0] : kx[1], iq = g < 2 ? iv[0] : iv[1];
            f32x4 av;
#pragma unroll
            for (int r = 0; r < 4; r++) {
                auto sw2 = __builtin_amdgcn_permlane32_swap(asu(mm[0][r]), asu(mm[1][r]), false, false);
                const float ga_ = asf(sw2[0]), ua = asf(sw2[1]);
                av[r] = ga_ * __builtin_amdgcn_rcpf(__builtin_amdgcn_exp2f(ga_ * kq) * iq + iq) * ua;
            }
            const u32 m0 = pk2(av[0], av[1]), m1 = pk2(av[2], av[3]);
            auto y0 = __builtin_amdgcn_permlane16_swap(m0, m0, false, false);
            auto y1 = __builtin_amdgcn_permlane16_swap(m1, m1, false, false);
            u32x4 v = {y0[0], y1[0], y0[1], y1[1]};
            if ((g & 1) == 0) st_sc1(rws, v, WS_ACT + (g >> 1) * (56 * 1024) + i * 16, (tt0 * 56 + 7 * a4 + 6) * 1024 + pp * 256);
        }
        STAMP(11);
        const u32 fdo = WS_FD + ((tb4 * 2 + hf) * 32 + fs) * 128;
        if (p3act) {
            SB();
            u32x2 pk0;
            if constexpr (PK0) pk0 = peek_issue<APOLL & 1>(rws, fd0, 15);
            SFOR(f, 28 - NWT5, 28) LDWTA(f); SEND
            SB();
            SFOR(k, 0, 7) aof[k] = (4 * tb * 56 + 7 * kw + K5(k)) * 1024; pins(aof[k]); SEND
            av[0][0] = WS_ACT + lane * 16 + ((wl & 1) ^ 1) * (56 * 1024); av[0][1] = WS_ACT + lane * 16 + (wl & 1) * (56 * 1024);
            av[1][0] = av[0][0] + 112 * 1024; av[1][1] = av[0][1] + 112 * 1024;
            fd1 = fd0 + 32 * 128;
            PIN(av[0][0]); PIN(av[0][1]); PIN(av[1][0]); PIN(av[1][1]); PIN(fd1);
            publish<NWT5 + PK0, 0>(rws, fdo, 1, tag);
            __builtin_amdgcn_s_setprio(0);
            bool ok = false;
            if constexpr (PK0) ok = peek_test<NWT5 + 1>(pk0, 15, tag);
            STCNT(56, (u32)ok);
            if (!ok) wait_flags<APOLL & 1>(rws, fd0, 15, tag);
        } else publish<0, 0>(rws, fdo, 1, tag);
    }

    if (__builtin_expect(p3act, 1)) {
        u32x4 af[2][2][7];
        u32x2 pk1;
        u32 lf, rb, da;
        char* op;
        f32x4 r0, r1;
#define AFLD(hb) do { SFOR(k, 0, 14) af[hb][k & 1][k >> 1] = rld(av[hb][k & 1], rws, aof[k >> 1]); SEND } while (0)
        STAMP(12);
        AFLD(0);
        SFOR(hb, 0, 2)
            const bool own = (wl >> 1) == hb;
            u32 pa[2][2];
            SFOR(k, 0, 4)
                const u32 tq = (wl & 1) ^ (k >> 1) ^ 1, nh = (k & 1) ^ hf;
                pa[k >> 1][k & 1] = (hb ? 0 : L_WO) + lane * 16 + ((tq * 4 + 2 * nh) * 7 + ((w - 2 * hb - tq - 4 * nh - 1) & 7)) * 1024;
                pinv(pa[k >> 1][k & 1]);
            SEND
            const u32 om = own ? ~0u : 0u;
            f32x4 ci[2] = {{bflo(hres[0] & om), bfhi(hres[0] & om), bflo(hres[1] & om), bfhi(hres[1] & om)},
                           {bflo(hres[2] & om), bfhi(hres[2] & om), bflo(hres[3] & om), bfhi(hres[3] & om)}};
            pinv(ci[0]); pinv(ci[1]);
            if constexpr (hb == 1) {
                rb = (wl >> 1 ? 0 : L_WO) + lane * 16 + ((wl & 1) * 4 + 2 * hf) * 7 * 1024;
                op = gout + (size_t)((64 * tb + 16 * wl + i) * 896 + (64 * nb + 32 * hf + 8 * (g >> 1) + 16 * (g & 1)) * 2);
                da = L_DONE + 4 * (lane & 7);
                pinv(rb); pinv(op); pinv(da);
            }
            SB();
            f32x4 acc[2][4];
            SFOR(k, 0, 14)
                constexpr int tl = k & 1, kk = k >> 1;
                if constexpr (hb == 0 && k == PK1) pk1 = peek_issue(rws, fd1, 15);
                if constexpr (hb == 1 && k == 11) lf = lds32(da);
                if constexpr (hb == 1 && k == 13) lds_spin(lf, da);
                aw<13 - k + (hb == 0 && k >= PK1)>(af[hb][tl][kk]);
#pragma unroll
                for (int t = 3; t >= 0; t--) acc[tl][t] = mfma(wd[4 * kk + t], af[hb][tl][kk], kk ? acc[tl][t] : tl && t < 2 ? ci[t] : (f32x4){0, 0, 0, 0});
                SFOR(t, 0, 4) pinv(acc[tl][t]); SEND
                SB();
            SEND
            if constexpr (hb == 0) {
                const bool ok = peek_test<0>(pk1, 15, tag);
                STCNT(57, (u32)ok);
                if (!ok) wait_flags(rws, fd1, 15, tag);
                STAMP(13);
                AFLD(1);
            }
            SFOR(k, 0, 6) ldsf4w(pa[k >> 2][(3 - (k & 3)) >> 1] + ((3 - (k & 3)) & 1) * 7168, acc[k >> 2][3 - (k & 3)]); SB(); SEND
            if (!own) { ldsf4w(pa[1][0] + 7168, acc[1][1]); ldsf4w(pa[1][0], acc[1][0]); }
            if (hb == 0 || own) { r0 = acc[1][0]; r1 = acc[1][1]; }
            STAMPG(16, 48 + 4 * hb);
        SEND
        BAR();
        STAMPG(16, 53);
        {
            f32x4 pr[14];
#pragma unroll
            for (int k = 0; k < 14; k++) pr[k] = ldsf4(rb + k * 1024);
            SB();
#pragma unroll
            for (int k = 0; k < 7; k++) r0 += pr[k];
#pragma unroll
            for (int k = 0; k < 7; k++) r1 += pr[7 + k];
            auto x0 = __builtin_amdgcn_permlane16_swap(pk2(r0[0], r0[1]), pk2(r1[0], r1[1]), false, false);
            auto x1 = __builtin_amdgcn_permlane16_swap(pk2(r0[2], r0[3]), pk2(r1[2], r1[3]), false, false);
            *(u32x4*)op = (u32x4){x0[0], x1[0], x0[1], x1[1]};
        }
        STAMP(14);
    }
    asm volatile(".Lfz_e0:" ::: "memory");
}

extern "C" void kernel_launch(void* const* d_in, const int* in_sizes, int n_in, void* d_out, int out_size, void* d_ws, size_t ws_size,
                              hipStream_t stream) {
    fused<<<256, 512, 0, stream>>>((const char*)d_in[0], (const char*)d_in[1], (const char*)d_in[2], (const char*)d_in[4],
                                   (const char*)d_in[5], (const char*)d_in[6], (char*)d_out, (char*)d_ws);
}
```
